```python
import torch

def _softmax(x, axis=-1):
    return torch.softmax(x, dim=axis)
def _gelu(x):
    return torch.nn.functional.gelu(x, approximate="tanh")
def _silu(x):
    return torch.nn.functional.silu(x)
def _sigmoid(x):
    return torch.sigmoid(x)
def _layernorm(x, axis=-1, eps=1e-5):
    m = x.mean(dim=axis, keepdim=True)
    v = x.var(dim=axis, keepdim=True, unbiased=False)
    return (x - m) / torch.sqrt(v + eps)
def _scatter_add(tgt, idx, src, axis=0):
    return tgt.clone().index_add_(axis, idx, src)
def _segment_sum(src, seg, n):
    out = torch.zeros((n,) + src.shape[1:], dtype=src.dtype)
    return out.index_add_(0, seg, src)
def _pad(x, pw):
    flat = tuple(v for pair in reversed(pw) for v in pair)
    return torch.nn.functional.pad(x, flat)

kernel_name = 'concat_heads__sliding_local_dilated__grouped_kv_mqa__prompt'

def setup_inputs():

    _w = (lambda a: (lambda t: t.to(torch.bfloat16) if t.is_floating_point() else t)(torch.as_tensor(a)))
    def _A_setup():
        B, S, D = 1, 2048, 576
        return {
            "x": _w((lambda *s: torch.randn(*s, dtype=torch.bfloat16) * 0.1)(B, S, D)),
            "wqkv": _w((lambda *s: torch.randn(*s, dtype=torch.bfloat16) * 0.8)(D, 3 * D)),
            "wo": _w((lambda *s: torch.randn(*s, dtype=torch.bfloat16) * 0.8)(D, D)),
        }
    _A_in = _A_setup()

    def _B_setup():
        B, S, D, H = 1, 2048, 576, 32
        Dh = D // H
        return {
            "x": _w((lambda *s: torch.randn(*s, dtype=torch.bfloat16) * 0.05)(B, S, D)),
            "wq": _w((lambda *s: torch.randn(*s, dtype=torch.bfloat16) * 3.2)(D, H * Dh)),
            "wk": _w((lambda *s: torch.randn(*s, dtype=torch.bfloat16) * 3.2)(D, Dh)),
            "wv": _w((lambda *s: torch.randn(*s, dtype=torch.bfloat16) * 1.6)(D, Dh)),
            "wo": _w((lambda *s: torch.randn(*s, dtype=torch.bfloat16) * 1.6)(H * Dh, D)),
        }
    _B_in = _B_setup()

    return {
        "A_x": _A_in["x"],
        "A_wqkv": _A_in["wqkv"],
        "A_wo": _A_in["wo"],
        "B_x": _B_in["x"],
        "B_wq": _B_in["wq"],
        "B_wk": _B_in["wk"],
        "B_wv": _B_in["wv"],
        "B_wo": _B_in["wo"],
    }

def reference(A_x, A_wqkv, A_wo, B_x, B_wq, B_wk, B_wv, B_wo):
    def _A_ref(x, wqkv, wo):
        B, S, D = x.shape
        H = 32; Dh = D // H
        W, dil = 8, 2
        qkv = (lambda x, *d: x.permute(*d))((x @ wqkv).reshape(B, S, 3, H, Dh), 2, 0, 3, 1, 4)
        q, k, v = qkv[0], qkv[1], qkv[2]
        scale = Dh ** -0.5
        pos = torch.arange(S)
        off = torch.arange(W) * dil
        idx = pos[:, None] - off[None, :]
        mask = idx >= 0
        idx_c = torch.where(mask, idx, 0).reshape(-1)
        kg = k[:, :, idx_c, :].reshape(B, H, S, W, Dh)
        vg = v[:, :, idx_c, :].reshape(B, H, S, W, Dh)
        logits = torch.einsum("bhsd,bhswd->bhsw", q, kg) * scale
        logits = torch.where(mask[None, None, :, :], logits, -1e9)
        attn = _softmax(logits, axis=-1)
        o = torch.einsum("bhsw,bhswd->bhsd", attn, vg)
        return (lambda x, *d: x.permute(*d))(o, 0, 2, 1, 3).reshape(B, S, D) @ wo

    def _B_ref(x, wq, wk, wv, wo):
        B, S, D = x.shape
        H = 32
        Dh = D // H
        q = (lambda x, *d: x.permute(*d))((x @ wq).reshape(B, S, H, Dh), 0, 2, 1, 3)
        k = x @ wk
        v = x @ wv
        scores = torch.einsum("bhqd,bkd->bhqk", q, k) * (Dh ** -0.5)
        p = _softmax(scores, axis=-1)
        o = torch.einsum("bhqk,bkd->bhqd", p, v)
        return (lambda x, *d: x.permute(*d))(o, 0, 2, 1, 3).reshape(B, S, H * Dh) @ wo

    a = _A_ref(A_x, A_wqkv, A_wo)
    b = _B_ref(B_x, B_wq, B_wk, B_wv, B_wo)
    return torch.concatenate([a, b], axis=-1)
```

```cpp
#include <hip/hip_runtime.h>
#include <hip/hip_bf16.h>
#include <stdint.h>

typedef __attribute__((ext_vector_type(8))) __bf16 bf16x8;
typedef __attribute__((ext_vector_type(4))) short s16x4;
typedef __attribute__((ext_vector_type(2))) __bf16 bf16x2;
typedef __attribute__((ext_vector_type(2))) float f32x2;
typedef __attribute__((ext_vector_type(16))) float f32x16;
typedef __attribute__((ext_vector_type(4))) float f32x4;
typedef __attribute__((ext_vector_type(4))) unsigned u32x4;
typedef __attribute__((ext_vector_type(2))) unsigned u32x2;
typedef unsigned short u16;
#define LAS __attribute__((address_space(3)))
#define DI __device__ __forceinline__

constexpr int S = 2048, D = 576, H = 32, DH = 18;
constexpr int TILE_B = 2496;
constexpr int K2_OFF = 1024, V_OFF = 1280;
constexpr float QSCALE = 0.23570226039551584f * 1.4426950408889634f;

DI unsigned pk2(float a, float b) { f32x2 v = {a, b}; bf16x2 r = __builtin_convertvector(v, bf16x2); return __builtin_bit_cast(unsigned, r); }
DI u16 bf1(float a) { return (u16)(pk2(a, 0.f) & 0xffffu); }
DI float bflo(unsigned u) { return __uint_as_float(u << 16); }
DI float bfhi(unsigned u) { return __uint_as_float(u & 0xffff0000u); }
DI s16x4 trread(const LAS char* p) { return __builtin_amdgcn_ds_read_tr16_b64_v4i16((LAS s16x4*)p); }
DI bf16x8 cat(s16x4 a, s16x4 b) { typedef __attribute__((ext_vector_type(8))) short s16x8; s16x8 v = __builtin_shufflevector(a, b, 0, 1, 2, 3, 4, 5, 6, 7); return __builtin_bit_cast(bf16x8, v); }

constexpr int XS = 144;
constexpr int WS_ = 208;
constexpr int XT_B = 128 * XS, WT_B = 64 * WS_, STG_B = XT_B + WT_B;
DI void gemm_tile(const u16* __restrict__ X, const u16* __restrict__ W, int ldw, int m0, int n0, u16* __restrict__ C, int ldc, int c0, float scale, char* smem) {
  const int tid = threadIdx.x, lane = tid & 63, wid = tid >> 6, wm = wid >> 1, wn = wid & 1;
  f32x4 acc[4][3];
#pragma unroll
  for (int i = 0; i < 4; ++i)
#pragma unroll
    for (int j = 0; j < 3; ++j) acc[i][j] = f32x4{0.f, 0.f, 0.f, 0.f};
  u32x4 xr[4], wr[3];
  auto gload = [&](int kc) {
#pragma unroll
    for (int i = 0; i < 4; ++i) { int idx = tid + 256 * i, r = idx >> 3, c = idx & 7; xr[i] = *(const u32x4*)(X + (size_t)(m0 + r) * D + kc * 64 + c * 8); }
#pragma unroll
    for (int i = 0; i < 3; ++i) { int idx = tid + 256 * i, r = idx / 12, c = idx % 12; wr[i] = *(const u32x4*)(W + (size_t)(kc * 64 + r) * ldw + n0 + c * 8); }
  };
  auto swrite = [&](char* buf) {
#pragma unroll
    for (int i = 0; i < 4; ++i) { int idx = tid + 256 * i, r = idx >> 3, c = idx & 7; *(u32x4*)(buf + r * XS + c * 16) = xr[i]; }
#pragma unroll
    for (int i = 0; i < 3; ++i) { int idx = tid + 256 * i, r = idx / 12, c = idx % 12; *(u32x4*)(buf + XT_B + r * WS_ + c * 16) = wr[i]; }
  };
  gload(0); swrite(smem); __syncthreads();
  const int g = lane >> 4, i16 = lane & 15, q = i16 >> 2, p = i16 & 3;
  for (int kc = 0; kc < 9; ++kc) {
    char* buf = smem + (kc & 1) * STG_B;
    if (kc + 1 < 9) gload(kc + 1);
#pragma unroll
    for (int ks = 0; ks < 2; ++ks) {
      bf16x8 xf[4], wf[3];
#pragma unroll
      for (int i = 0; i < 4; ++i) xf[i] = *(const bf16x8*)(buf + (wm * 64 + i * 16 + i16) * XS + ks * 64 + g * 16);
#pragma unroll
      for (int j = 0; j < 3; ++j) {
        const LAS char* wp = (const LAS char*)(buf + XT_B + (ks * 32 + 8 * g + q) * WS_ + (wn * 48 + j * 16 + 4 * p) * 2);
        wf[j] = cat(trread(wp), trread(wp + 4 * WS_));
      }
#pragma unroll
      for (int i = 0; i < 4; ++i)
#pragma unroll
        for (int j = 0; j < 3; ++j) acc[i][j] = __builtin_amdgcn_mfma_f32_16x16x32_bf16(wf[j], xf[i], acc[i][j], 0, 0, 0);
    }
    if (kc + 1 < 9) swrite(smem + ((kc + 1) & 1) * STG_B);
    __syncthreads();
  }
#pragma unroll
  for (int i = 0; i < 4; ++i)
#pragma unroll
    for (int j = 0; j < 3; ++j) {
      u32x2 v = {pk2(acc[i][j][0] * scale, acc[i][j][1] * scale), pk2(acc[i][j][2] * scale, acc[i][j][3] * scale)};
      *(u32x2*)(C + (size_t)(m0 + wm * 64 + i * 16 + i16) * ldc + c0 + wn * 48 + j * 16 + 4 * g) = v;
    }
}

DI void kv_proj(const u16* __restrict__ X, const u16* __restrict__ wk, const u16* __restrict__ wv, char* __restrict__ img, int g8, char* smem) {
  const int tid = threadIdx.x, lane = tid & 63, wid = tid >> 6;
  for (int idx = tid; idx < 576 * 20; idx += 256) {
    int row = idx / 20, c = idx % 20; unsigned v = 0;
    if (c < 9) v = *(const unsigned*)(wk + row * 18 + 2 * c); else if (c >= 10 && c < 19) v = *(const unsigned*)(wv + row * 18 + 2 * (c - 10));
    *(unsigned*)(smem + idx * 4) = v;
  }
  if (tid < 8) *(unsigned*)(smem + 46080 + tid * 4) = 0;
  __syncthreads();
  const int g = lane >> 4, i16 = lane & 15, q = i16 >> 2, p = i16 & 3;
  f32x4 acc[3];
#pragma unroll
  for (int j = 0; j < 3; ++j) acc[j] = f32x4{0.f, 0.f, 0.f, 0.f};
  for (int ks = wid; ks < 18; ks += 4) {
    bf16x8 xf = *(const bf16x8*)(X + (size_t)(g8 * 8 + (i16 & 7)) * D + ks * 32 + g * 8);
#pragma unroll
    for (int j = 0; j < 3; ++j) {
      const LAS char* wp = (const LAS char*)(smem + (ks * 32 + 8 * g + q) * 80 + (j * 16 + 4 * p) * 2);
      acc[j] = __builtin_amdgcn_mfma_f32_16x16x32_bf16(cat(trread(wp), trread(wp + 4 * 80)), xf, acc[j], 0, 0, 0);
    }
  }
  float* part = (float*)(smem + 46336);
  if (i16 < 8) {
#pragma unroll
    for (int j = 0; j < 3; ++j)
#pragma unroll
      for (int r = 0; r < 4; ++r) part[(wid * 48 + j * 16 + 4 * g + r) * 8 + i16] = acc[j][r];
  }
  __syncthreads();
  for (int t = tid; t < 320; t += 256) {
    int n = t >> 3, j = t & 7;
    float v = part[n * 8 + j] + part[(48 + n) * 8 + j] + part[(96 + n) * 8 + j] + part[(144 + n) * 8 + j];
    int key = g8 * 8 + j, tile = key >> 5, kk = key & 31, s = kk >> 4, h = (kk >> 3) & 1;
    int rho = 16 * s + 8 * (j >> 2) + 4 * h + (j & 3);
    char* base = img + (size_t)tile * TILE_B;
    if (n < 8) *(u16*)(base + rho * 16 + n * 2) = bf1(v);
    else if (n < 10) *(u16*)(base + K2_OFF + rho * 8 + (n - 8) * 2) = bf1(v);
    else if (n < 18) *(u16*)(base + 512 + rho * 16 + (n - 10) * 2) = bf1(v);
    else if (n < 20) *(u16*)(base + K2_OFF + rho * 8 + (n - 16) * 2) = 0x3f80;
    else if (n < 38) *(u16*)(base + V_OFF + (s * 2 + h) * 304 + (n - 20) * 16 + j * 2) = bf1(v);
    else if (n == 38) *(u16*)(base + V_OFF + (s * 2 + h) * 304 + 18 * 16 + j * 2) = 0x3f80;
  }
}

__global__ __launch_bounds__(256) void k_proj(const u16* __restrict__ Ax, const u16* __restrict__ Awqkv, const u16* __restrict__ Bx, const u16* __restrict__ Bwq,
                                              const u16* __restrict__ Bwk, const u16* __restrict__ Bwv, u16* __restrict__ Aqkv, u16* __restrict__ Bq, char* __restrict__ img) {
  extern __shared__ __attribute__((aligned(16))) char smem[];
  int b = blockIdx.x;
  if (b < 256) { kv_proj(Bx, Bwk, Bwv, img, b, smem); return; }
  b -= 256;
  int mt = b & 15, nt = b >> 4;
  if (nt < 18) gemm_tile(Ax, Awqkv, 3 * D, mt * 128, nt * 96, Aqkv, 3 * D, nt * 96, 1.f, smem);
  else gemm_tile(Bx, Bwq, D, mt * 128, (nt - 18) * 96, Bq, D, (nt - 18) * 96, QSCALE, smem);
}

__global__ __launch_bounds__(256) void k_out(const u16* __restrict__ oA, const u16* __restrict__ Awo, const u16* __restrict__ oB, const u16* __restrict__ Bwo, u16* __restrict__ out) {
  extern __shared__ __attribute__((aligned(16))) char smem[];
  int b = blockIdx.x, mt = b & 15, nt = b >> 4;
  if (nt < 6) gemm_tile(oA, Awo, D, mt * 128, nt * 96, out, 2 * D, nt * 96, 1.f, smem);
  else gemm_tile(oB, Bwo, D, mt * 128, (nt - 6) * 96, out, 2 * D, D + (nt - 6) * 96, 1.f, smem);
}

DI void local_attn(const u16* __restrict__ qkv, u16* __restrict__ oA, int s, int h) {
  const unsigned* qp = (const unsigned*)(qkv + (size_t)s * 3 * D + h * DH);
  float qf[18];
#pragma unroll
  for (int i = 0; i < 9; ++i) { unsigned u = qp[i]; qf[2 * i] = bflo(u); qf[2 * i + 1] = bfhi(u); }
  float lg[8], mx = -3.0e38f;
#pragma unroll
  for (int w = 0; w < 8; ++w) {
    int j = s - 2 * w, jc = j < 0 ? 0 : j;
    const unsigned* kp = (const unsigned*)(qkv + (size_t)jc * 3 * D + D + h * DH);
    float a = 0.f;
#pragma unroll
    for (int i = 0; i < 9; ++i) { unsigned u = kp[i]; a = fmaf(qf[2 * i], bflo(u), a); a = fmaf(qf[2 * i + 1], bfhi(u), a); }
    a *= 0.23570226039551584f;
    lg[w] = j < 0 ? -1e9f : a;
    mx = fmaxf(mx, lg[w]);
  }
  float l = 0.f, o[18];
#pragma unroll
  for (int i = 0; i < 18; ++i) o[i] = 0.f;
#pragma unroll
  for (int w = 0; w < 8; ++w) {
    float p = __builtin_amdgcn_exp2f((lg[w] - mx) * 1.4426950408889634f); l += p;
    int j = s - 2 * w, jc = j < 0 ? 0 : j;
    const unsigned* vp = (const unsigned*)(qkv + (size_t)jc * 3 * D + 2 * D + h * DH);
#pragma unroll
    for (int i = 0; i < 9; ++i) { unsigned u = vp[i]; o[2 * i] = fmaf(p, bflo(u), o[2 * i]); o[2 * i + 1] = fmaf(p, bfhi(u), o[2 * i + 1]); }
  }
  float r = 1.f / l;
  unsigned* op = (unsigned*)(oA + (size_t)s * D + h * DH);
#pragma unroll
  for (int i = 0; i < 9; ++i) op[i] = pk2(o[2 * i] * r, o[2 * i + 1] * r);
}

#define MFMA16(a, b, c) __builtin_amdgcn_mfma_f32_32x32x16_bf16(a, b, c, 0, 0, 0)
#define MFMA8(a, b, c) __builtin_amdgcn_mfma_f32_32x32x8bf16_1k(a, b, c, 0, 0, 0)

__global__ __launch_bounds__(512, 2) void k_attn(const u16* __restrict__ Bq, const char* __restrict__ img, u16* __restrict__ oB, const u16* __restrict__ Aqkv, u16* __restrict__ oA) {
  extern __shared__ __attribute__((aligned(16))) char smem[];
  const int tid = threadIdx.x, lane = tid & 63, wid = __builtin_amdgcn_readfirstlane(tid >> 6), r32 = lane & 31, hi = lane >> 5;
  const int b = blockIdx.x, xcd = b & 7, jj = b >> 3, rb = xcd * 4 + (jj >> 3), hg = jj & 7;
  for (int i = wid; i < 156; i += 8)
    __builtin_amdgcn_global_load_lds((const __attribute__((address_space(1))) void*)(img + i * 1024 + lane * 16), (LAS void*)(smem + i * 1024), 16, 0, 0);
  if (tid < 256) local_attn(Aqkv, oA, b * 8 + (tid >> 5), tid & 31);
  const int head = hg * 4 + (wid >> 1), qrow = rb * 64 + (wid & 1) * 32 + r32;
  const unsigned* qp = (const unsigned*)(Bq + (size_t)qrow * D + head * DH);
  u32x4 q1u; u32x2 q2u;
  q1u[0] = qp[hi * 5 + 0]; q1u[1] = qp[hi * 5 + 1]; q1u[2] = qp[hi * 5 + 2]; q1u[3] = qp[hi * 5 + 3];
  q2u[0] = hi ? 0u : qp[4]; q2u[1] = 0u;
  const bf16x8 q1 = __builtin_bit_cast(bf16x8, q1u);
  __syncthreads();
  const char* kp = smem + hi * 512 + r32 * 16;
  const char* k2p = smem + K2_OFF + r32 * 8;
  const char* vp = smem + V_OFF + hi * 304 + (r32 < 19 ? r32 : (r32 & 3)) * 16;
  f32x16 zero;
#pragma unroll
  for (int i = 0; i < 16; ++i) zero[i] = 0.f;
  auto scores = [&](int t, u32x2 q2) {
    bf16x8 kf = *(const bf16x8*)(kp + t * TILE_B); s16x4 k2 = *(const s16x4*)(k2p + t * TILE_B);
    f32x16 s = MFMA16(kf, q1, zero);
    return MFMA8(k2, __builtin_bit_cast(s16x4, q2), s);
  };
  auto rowmax = [&](int t0, int t1) {
    float m = -3.0e38f;
    for (int t = t0; t < t1; ++t) { f32x16 s = scores(t, q2u);
#pragma unroll
      for (int i = 0; i < 16; i += 2) m = fmaxf(fmaxf(m, s[i]), s[i + 1]); }
    auto rr = __builtin_amdgcn_permlane32_swap(__float_as_uint(m), __float_as_uint(m), false, false);
    return fmaxf(__uint_as_float(rr[0]), __uint_as_float(rr[1]));
  };
  f32x16 o;
  {
    float m = rowmax(0, 2);
    unsigned mh = pk2(-m, 0.f) & 0xffffu; float rem = -m - bflo(mh);
    u32x2 q2 = q2u; q2[1] = hi ? 0u : (mh | (pk2(0.f, rem) & 0xffff0000u));
    o = zero;
#pragma unroll 2
    for (int t = 0; t < 64; ++t) {
      f32x16 s = scores(t, q2);
      bf16x8 v0 = *(const bf16x8*)(vp + t * TILE_B), v1 = *(const bf16x8*)(vp + t * TILE_B + 608);
      u32x4 p0, p1;
#pragma unroll
      for (int i = 0; i < 4; ++i) p0[i] = pk2(__builtin_amdgcn_exp2f(s[2 * i]), __builtin_amdgcn_exp2f(s[2 * i + 1]));
      o = MFMA16(v0, __builtin_bit_cast(bf16x8, p0), o);
#pragma unroll
      for (int i = 0; i < 4; ++i) p1[i] = pk2(__builtin_amdgcn_exp2f(s[8 + 2 * i]), __builtin_amdgcn_exp2f(s[8 + 2 * i + 1]));
      o = MFMA16(v1, __builtin_bit_cast(bf16x8, p1), o);
    }
  }
  float l = o[10];
  { auto rr = __builtin_amdgcn_permlane32_swap(__float_as_uint(l), __float_as_uint(l), false, false); l = __uint_as_float(rr[0]); }
  float amax = fmaxf(fmaxf(fmaxf(fabsf(o[0]), fabsf(o[1])), fmaxf(fabsf(o[2]), fabsf(o[3]))), fmaxf(fmaxf(fabsf(o[4]), fabsf(o[5])), fmaxf(fabsf(o[6]), fabsf(o[7]))));
  if (!hi) amax = fmaxf(amax, fmaxf(fabsf(o[8]), fabsf(o[9])));
  const bool ok = (l > 0.25f) && (l < 1.0e37f) && (amax < 1.0e37f);
  if (__builtin_amdgcn_readfirstlane(__any(!ok))) {
    float m = rowmax(0, 64);
    o = zero;
    for (int t = 0; t < 64; ++t) {
      f32x16 s = scores(t, q2u);
      bf16x8 v0 = *(const bf16x8*)(vp + t * TILE_B), v1 = *(const bf16x8*)(vp + t * TILE_B + 608);
      u32x4 p0, p1;
#pragma unroll
      for (int i = 0; i < 4; ++i) p0[i] = pk2(__builtin_amdgcn_exp2f(s[2 * i] - m), __builtin_amdgcn_exp2f(s[2 * i + 1] - m));
      o = MFMA16(v0, __builtin_bit_cast(bf16x8, p0), o);
#pragma unroll
      for (int i = 0; i < 4; ++i) p1[i] = pk2(__builtin_amdgcn_exp2f(s[8 + 2 * i] - m), __builtin_amdgcn_exp2f(s[8 + 2 * i + 1] - m));
      o = MFMA16(v1, __builtin_bit_cast(bf16x8, p1), o);
    }
    l = o[10];
    auto rr = __builtin_amdgcn_permlane32_swap(__float_as_uint(l), __float_as_uint(l), false, false); l = __uint_as_float(rr[0]);
  }
  const float rl = 1.f / l;
  unsigned* op = (unsigned*)(oB + (size_t)qrow * D + head * DH);
  op[2 * hi + 0] = pk2(o[0] * rl, o[1] * rl); op[2 * hi + 1] = pk2(o[2] * rl, o[3] * rl);
  op[4 + 2 * hi + 0] = pk2(o[4] * rl, o[5] * rl); op[4 + 2 * hi + 1] = pk2(o[6] * rl, o[7] * rl);
  if (!hi) op[8] = pk2(o[8] * rl, o[9] * rl);
}

extern "C" void kernel_launch(void* const* d_in, const int* in_sizes, int n_in, void* d_out, int out_size, void* d_ws, size_t ws_size, hipStream_t stream) {
  const u16* Ax = (const u16*)d_in[0]; const u16* Awqkv = (const u16*)d_in[1]; const u16* Awo = (const u16*)d_in[2];
  const u16* Bx = (const u16*)d_in[3]; const u16* Bwq = (const u16*)d_in[4]; const u16* Bwk = (const u16*)d_in[5]; const u16* Bwv = (const u16*)d_in[6]; const u16* Bwo = (const u16*)d_in[7];
  char* ws = (char*)d_ws;
  u16* Aqkv = (u16*)ws; u16* Bq = (u16*)(ws + (8u << 20)); u16* oA = (u16*)(ws + (12u << 20)); u16* oB = (u16*)(ws + (16u << 20)); char* img = ws + (20u << 20);
  hipFuncSetAttribute((const void*)k_attn, hipFuncAttributeMaxDynamicSharedMemorySize, 160 * 1024);
  hipFuncSetAttribute((const void*)k_proj, hipFuncAttributeMaxDynamicSharedMemorySize, 2 * STG_B);
  hipFuncSetAttribute((const void*)k_out, hipFuncAttributeMaxDynamicSharedMemorySize, 2 * STG_B);
  k_proj<<<256 + 16 * 24, 256, 2 * STG_B, stream>>>(Ax, Awqkv, Bx, Bwq, Bwk, Bwv, Aqkv, Bq, img);
  k_attn<<<256, 512, 64 * TILE_B, stream>>>(Bq, img, oB, Aqkv, oA);
  k_out<<<16 * 12, 256, 2 * STG_B, stream>>>(oA, Awo, oB, Bwo, (u16*)d_out);
}
```

```cpp
#include <hip/hip_runtime.h>
#include <hip/hip_bf16.h>
#include <stdint.h>

typedef __attribute__((ext_vector_type(8))) __bf16 bf16x8;
typedef __attribute__((ext_vector_type(4))) short s16x4;
typedef __attribute__((ext_vector_type(8))) short s16x8;
typedef __attribute__((ext_vector_type(2))) __bf16 bf16x2;
typedef __attribute__((ext_vector_type(2))) float f32x2;
typedef __attribute__((ext_vector_type(16))) float f32x16;
typedef __attribute__((ext_vector_type(4))) float f32x4;
typedef __attribute__((ext_vector_type(4))) unsigned u32x4;
typedef __attribute__((ext_vector_type(2))) unsigned u32x2;
typedef unsigned short u16;
#define LAS __attribute__((address_space(3)))
#define GAS __attribute__((address_space(1)))
#define DI __device__ __forceinline__
#define SB() __builtin_amdgcn_sched_barrier(0)
#define BC8(x) __builtin_bit_cast(bf16x8, x)
#define BC4(x) __builtin_bit_cast(s16x4, x)
#define MFMA16(a, b, c) __builtin_amdgcn_mfma_f32_32x32x16_bf16(a, b, c, 0, 0, 0)
#define MFMA8(a, b, c) __builtin_amdgcn_mfma_f32_32x32x8bf16_1k(a, b, c, 0, 0, 0)
#define MF16(a, b, c) __builtin_amdgcn_mfma_f32_16x16x32_bf16(a, b, c, 0, 0, 0)
#define EX(x) __builtin_amdgcn_exp2f(x)
#define WAITV(n) asm volatile("s_waitcnt vmcnt(" #n ")" ::: "memory")
#define BAR() asm volatile("s_waitcnt lgkmcnt(0)\n\ts_barrier" ::: "memory")
#define TS(i)

constexpr int S = 2048, D = 576, H = 32, DH = 18;
constexpr float QSCALE = 0.23570226039551584f * 1.4426950408889634f;
constexpr int BLK_B = 640, TILE_B = 4 * BLK_B, K2_OFF = 256, V_OFF = 320;
constexpr size_t WS_EPOCH = 0, WS_KVFLAG = 1024, WS_OAFLAG = 2048, WS_OBFLAG = 3072, WS_IMG = 4096, WS_OA = 262144, WS_OB = WS_OA + (size_t)S * D * 2;
constexpr int SPIN_MAX = 1 << 17;
constexpr int L_R0 = 0, L_XB = 46080, L_WQ0 = 119808, L_D = 156672, L_MISC = 161792, WV_OFF = 21504;
constexpr int L_QX = 149504;
constexpr int L_XA = 0, L_RING = 92160, RING_B = 13824, L_F32 = 0, F32_B = 23040;
constexpr int L_WO = 0, L_STG_A = 92160, L_OUT_A = 101376;
constexpr int L_CMB = 83968, L_STG_B = 133120, L_RED = 83968, L_OUT_B = 142336;

DI unsigned pk2(float a, float b) { f32x2 v = {a, b}; bf16x2 r = __builtin_convertvector(v, bf16x2); return __builtin_bit_cast(unsigned, r); }
DI float bflo(unsigned u) { return __uint_as_float(u << 16); }
DI s16x4 trd(unsigned a) { return __builtin_amdgcn_ds_read_tr16_b64_v4i16((LAS s16x4*)(uintptr_t)a); }
DI bf16x8 cat(s16x4 a, s16x4 b) { s16x8 v = __builtin_shufflevector(a, b, 0, 1, 2, 3, 4, 5, 6, 7); return BC8(v); }
template <class T> DI T lds_ld(unsigned a) { return *(const LAS T*)(uintptr_t)a; }
template <class T> DI void lds_st(unsigned a, T v) { *(LAS T*)(uintptr_t)a = v; }
DI u32x4 mk_rsrc(const void* p, unsigned bytes) { unsigned long long a = (unsigned long long)p; return u32x4{(unsigned)a, (unsigned)(a >> 32) & 0xffffu, bytes, 0x00020000u}; }
DI __amdgpu_buffer_rsrc_t mk_rs(const void* p, unsigned bytes) { return __builtin_amdgcn_make_buffer_rsrc((void*)p, 0, bytes, 0x00020000); }
DI void dma16(u32x4 rs, unsigned lds, unsigned voff, unsigned soff) {
  asm volatile("s_mov_b32 m0, %0\n\ts_nop 0\n\tbuffer_load_dwordx4 %1, %2, %3 offen lds" :: "s"(lds), "v"(voff), "s"(rs), "s"(soff) : "memory");
}
DI void dma16_lo(u32x4 rs, unsigned lds, unsigned voff, unsigned soff) {
  asm volatile("s_mov_b32 m0, %0\n\ts_mov_b64 exec, 0xffffffff\n\tbuffer_load_dwordx4 %1, %2, %3 offen lds\n\ts_mov_b64 exec, -1" :: "s"(lds), "v"(voff), "s"(rs), "s"(soff) : "memory");
}
DI float swapmax(float m) { auto rr = __builtin_amdgcn_permlane32_swap(__float_as_uint(m), __float_as_uint(m), false, false); return fmaxf(__uint_as_float(rr[0]), __uint_as_float(rr[1])); }
DI float lowhalf(float m) { auto rr = __builtin_amdgcn_permlane32_swap(__float_as_uint(m), __float_as_uint(m), false, false); return __uint_as_float(rr[0]); }
DI unsigned xt_voff(int lane, int octpar) { return (unsigned)((lane >> 3) * (D * 2) + (((lane & 7) ^ ((4 * octpar + (lane >> 4)) & 7)) << 4)); }
DI unsigned ws_voff(int lane, int i) { int idx = 64 * i + lane, row = (idx * 7282) >> 16, pc = idx - row * 9; return (unsigned)(row * (D * 2) + pc * 16); }
DI void wait_flags(const char* flags, int n, unsigned tag, int lane) {
  const __amdgpu_buffer_rsrc_t rf = mk_rs(flags, n * 4);
  const bool live = lane * 4 < n;
#pragma unroll 1
  for (int spin = 0; spin < SPIN_MAX; ++spin) {
    u32x4 f = __builtin_amdgcn_raw_buffer_load_b128(rf, lane * 16, 0, 16);
    if (__all(!live || (f[0] == tag && f[1] == tag && f[2] == tag && f[3] == tag))) break;
    __builtin_amdgcn_s_sleep(1);
  }
  asm volatile("" ::: "memory");
}
DI void aux_sync(unsigned cnt, unsigned target, int lane) {
  asm volatile("s_waitcnt lgkmcnt(0)" ::: "memory");
  if (lane == 0) __hip_atomic_fetch_add((LAS unsigned*)(uintptr_t)cnt, 1u, __ATOMIC_RELAXED, __HIP_MEMORY_SCOPE_WORKGROUP);
#pragma unroll 1
  for (int spin = 0; spin < SPIN_MAX; ++spin) {
    unsigned v = __hip_atomic_load((LAS unsigned*)(uintptr_t)cnt, __ATOMIC_RELAXED, __HIP_MEMORY_SCOPE_WORKGROUP);
    if (__builtin_amdgcn_readfirstlane(v) >= target) break;
    __builtin_amdgcn_s_sleep(1);
  }
  asm volatile("" ::: "memory");
}

DI void fmac(float& acc, float a, float b) { asm("v_fmac_f32 %0, %1, %2" : "+v"(acc) : "v"(a), "v"(b)); }
DI void Cv(u32x4& p, int i, const f32x4& e) { unsigned x_ = pk2(e[0], e[1]), y_ = pk2(e[2], e[3]); asm volatile("" : "+v"(x_), "+v"(y_)); p[i] = x_; p[i + 1] = y_; SB(); }
DI void Ev(f32x4& e, const f32x16& s, int j) { e[0] = EX(s[j]); e[1] = EX(s[j + 1]); e[2] = EX(s[j + 2]); e[3] = EX(s[j + 3]); asm volatile("" : "+v"(e)); SB(); }
DI void step(f32x16& sn, const f32x16& sc, f32x16& oprev, f32x16& o, u32x4& p1, f32x4& e, u32x4 kf, u32x2 k2, u32x4 q1, u32x2 q2, u32x4 v1prev, u32x4 v0, const f32x16& zero) {
  u32x4 p0;
  sn = MFMA16(BC8(kf), BC8(q1), zero); SB();
  Cv(p1, 2, e); Ev(e, sc, 0);
  sn = MFMA8(BC4(k2), BC4(q2), sn); SB();
  Cv(p0, 0, e); Ev(e, sc, 4);
  oprev = MFMA16(BC8(v1prev), BC8(p1), oprev); SB();
  Cv(p0, 2, e); Ev(e, sc, 8);
  o = MFMA16(BC8(v0), BC8(p0), o); SB();
  Cv(p1, 0, e); Ev(e, sc, 12);
}

DI void publish_o(char* oimg, int rb, int hg, unsigned stg, int i0, int di, int lane) {
  const __amdgpu_buffer_rsrc_t ro = mk_rs(oimg + (size_t)rb * 73728 + hg * 2304, 73728);
  for (int i = i0; i < 9; i += di) {
    const int idx = 64 * i + lane, m16 = (idx * 3641) >> 19;
    __builtin_amdgcn_raw_buffer_store_b128(lds_ld<u32x4>(stg + idx * 16), ro, (unsigned)(m16 * (18432 - 2304) + idx * 16), 0, 16);
  }
  WAITV(0);
}
DI void store_out(u16* outp, unsigned stg, int c0, int dc) {
  for (int c = c0; c < 576; c += dc) { const int r = (c * 7282) >> 16, pc = c - r * 9;
    *(u32x4*)(outp + (size_t)r * (2 * D) + pc * 8) = lds_ld<u32x4>(stg + r * 160 + pc * 16); }
}

struct Args { const u16* Ax; const u16* Awqkv; const u16* Awo; const u16* Bx; const u16* Bwq; const u16* Bwk; const u16* Bwv; const u16* Bwo; char* ws; u16* out; };

__global__ __launch_bounds__(512, 2) void k_fused(Args A) {
  extern __shared__ __attribute__((aligned(16))) char smem[];
  const unsigned L0 = (unsigned)(uintptr_t)(LAS char*)smem;
  char* const ws = A.ws;
  const int tid = threadIdx.x, lane = tid & 63, wid = __builtin_amdgcn_readfirstlane(tid >> 6), r32 = lane & 31, hi = lane >> 5;
  const int g4 = lane >> 4, i16 = lane & 15, tq = i16 >> 2, tp = i16 & 3;
  const int b = blockIdx.x, xcd = b & 7, jj = b >> 3, rb = xcd * 4 + (jj >> 3), hg = jj & 7, g8 = rb * 8 + hg;
  TS(0);
  unsigned tag;
  { const u32x4 rs_e = mk_rsrc(ws + WS_EPOCH, 4); const unsigned z = 0; asm volatile("buffer_load_dword %0, %1, %2, 0 offen" : "=v"(tag) : "v"(z), "s"(rs_e) : "memory"); }
  const u32x4 rs_x = mk_rsrc(A.Bx + (size_t)rb * 64 * D, 64 * D * 2), rs_wq = mk_rsrc(A.Bwq + hg * 72, (D * D - hg * 72) * 2);
  const unsigned wvw = ws_voff(lane, wid), wv8 = ws_voff(lane, 8);
  {
    const u32x4 rs_wk = mk_rsrc(A.Bwk, D * DH * 2), rs_wv = mk_rsrc(A.Bwv, D * DH * 2);
    const unsigned xvh = xt_voff(lane, hg & 1);
    for (int kc = wid; kc < 9; kc += 8) dma16(rs_x, L0 + L_XB + (kc * 8 + hg) * 1024, xvh, hg * 8 * D * 2 + kc * 128);
    for (int i = wid; i < 21; i += 8) { dma16(rs_wk, L0 + L_R0 + i * 1024, lane * 16, i * 1024); dma16(rs_wv, L0 + L_R0 + WV_OFF + i * 1024, lane * 16, i * 1024); }
    if (wid < 7) {
      const int oc = (hg + 1 + wid) & 7; const unsigned xv = xt_voff(lane, oc & 1);
      for (int kc = 0; kc < 9; ++kc) {
        dma16(rs_x, L0 + L_XB + (kc * 8 + oc) * 1024, xv, oc * 8 * D * 2 + kc * 128);
        if (kc < 4) dma16(rs_wq, L0 + L_WQ0 + kc * 9216 + wid * 1024, wvw, kc * 64 * D * 2);
      }
      WAITV(13);
    } else {
      for (int kc = 0; kc < 4; ++kc) { dma16(rs_wq, L0 + L_WQ0 + kc * 9216 + 7 * 1024, wvw, kc * 64 * D * 2); dma16(rs_wq, L0 + L_WQ0 + kc * 9216 + 8 * 1024, wv8, kc * 64 * D * 2); }
      if (lane < 16) lds_st<unsigned>(L0 + L_MISC + lane * 4, 0u);
      WAITV(8);
    }
    asm volatile("" : "+v"(tag));
    tag += 1u;
  }
  TS(1);
  BAR();
  TS(2);
  if (wid < 5) {
    int G = 4 * wid + tp; G = G > 17 ? 17 : G;
    const unsigned wa = L0 + L_R0 + (8 * g4 + tq) * 72 + G * 8 + (G >= 9 ? WV_OFF - 72 : 0);
    const int row = hg * 8 + (i16 & 7); const unsigned sw = (row >> 1) & 7;
    const unsigned xa0 = L0 + L_XB + row * 128 + (((2 * g4) ^ sw) << 4), xa1 = xa0 ^ 16;
    const unsigned sel = (i16 & 8) ? 0x07060302u : 0x05040100u;
    f32x4 acc = {0.f, 0.f, 0.f, 0.f};
#pragma unroll
    for (int ks = 0; ks < 9; ++ks) {
      const u32x4 x0 = lds_ld<u32x4>(xa0 + ks * 8192), x1 = lds_ld<u32x4>(xa1 + ks * 8192);
      u32x4 xf = {__builtin_amdgcn_perm(x0[1], x0[0], sel), __builtin_amdgcn_perm(x0[3], x0[2], sel), __builtin_amdgcn_perm(x1[1], x1[0], sel), __builtin_amdgcn_perm(x1[3], x1[2], sel)};
      acc = MF16(cat(trd(wa + ks * 2304), trd(wa + ks * 2304 + 288)), BC8(xf), acc);
    }
    lds_st<f32x4>(L0 + L_D + (i16 * 80 + 16 * wid + 4 * g4) * 4, acc);
  }
  BAR();
  TS(3);
  if (wid == 7) {
    int a0, st;
    if (lane < 16) { a0 = (lane >> 1) * 80 + (lane & 1) * 10; st = 1; }
    else if (lane < 20) { a0 = (lane - 16) * 160 + 8; st = 1; }
    else { int d = lane - 20; d = d > 17 ? 17 : d; a0 = 36 + d; st = 80; }
    const bool k2c = (lane & 60) == 16;
    float v[8];
#pragma unroll
    for (int e = 0; e < 8; ++e) {
      int idx = a0 + e * st; if (k2c && e >= 4) idx += 76;
      v[e] = lds_ld<float>(L0 + L_D + idx * 4) + lds_ld<float>(L0 + L_D + (idx + 658) * 4);
    }
    u32x4 c = {pk2(v[0], v[1]), pk2(v[2], v[3]), pk2(v[4], v[5]), pk2(v[6], v[7])};
    if (k2c || lane == 38) { c[1] = 0x3f803f80u; c[3] = 0x3f803f80u; }
    if (lane == 38) { c[0] = 0x3f803f80u; c[2] = 0x3f803f80u; }
    __builtin_amdgcn_raw_buffer_store_b128(c, mk_rs(ws + WS_IMG + (size_t)g8 * BLK_B, BLK_B), lane * 16, 0, 16);
    WAITV(0);
    if (lane == 0) __hip_atomic_store((GAS unsigned*)(ws + WS_KVFLAG) + g8, tag, __ATOMIC_RELAXED, __HIP_MEMORY_SCOPE_AGENT);
  }
  TS(4);
  { const int kx = wid < 4 ? wid : 4;
    dma16(rs_wq, L0 + L_R0 + kx * 9216 + 8 * 1024, wv8, (kx + 4) * 64 * D * 2);
    for (int kc = 0; kc < 5; ++kc) dma16(rs_wq, L0 + L_R0 + kc * 9216 + wid * 1024, wvw, (kc + 4) * 64 * D * 2); }
  const int hl = wid >> 1, qh = wid & 1, par = hl & 1;
  f32x16 zero;
#pragma unroll
  for (int i = 0; i < 16; ++i) zero[i] = 0.f;
  u32x4 q1o; u32x2 q2o;
  {
    f32x16 qa = zero;
    const int gg = 2 * (g4 & 1) + (tp >> 1), hh = tp & 1;
    int col = ((hl * 18) & ~3) + (hh ? (par ? 12 : 8) : 0) + 4 * gg; col = col > 68 ? 68 : col;
    unsigned wb = L0 + L_WQ0 + (8 * hi + tq) * 144 + col * 2;
    const int row = qh * 32 + r32; const unsigned sw = (row >> 1) & 7;
    unsigned xo = L0 + L_XB + row * 128 + ((hi ^ sw) << 4);
#pragma unroll 1
    for (int kc = 0; kc < 9; ++kc) {
      if (kc == 0) { WAITV(15); BAR(); } else if (kc == 2) { WAITV(11); BAR(); } else if (kc == 4) { WAITV(2); BAR(); wb -= L_WQ0 + 4 * 9216 - L_R0; } else if (kc == 7) { WAITV(0); BAR(); }
      s16x4 wf[8]; bf16x8 xf[4];
#pragma unroll
      for (int s = 0; s < 4; ++s) { wf[2 * s] = trd(wb + s * 2304); wf[2 * s + 1] = trd(wb + s * 2304 + 576); xf[s] = lds_ld<bf16x8>(xo ^ (s << 5)); }
      SB();
#pragma unroll
      for (int s = 0; s < 4; ++s) qa = MFMA16(cat(wf[2 * s], wf[2 * s + 1]), xf[s], qa);
      wb += 9216; xo += 8192;
    }
    unsigned pk[6];
#pragma unroll
    for (int i = 0; i < 6; ++i) pk[i] = pk2(qa[2 * i] * QSCALE, qa[2 * i + 1] * QSCALE);
    const bool o1 = (par ^ hi) != 0;
#pragma unroll
    for (int i = 0; i < 4; ++i) q1o[i] = o1 ? pk[i + 1] : pk[i];
    q2o[0] = hi ? 0u : (o1 ? pk[5] : pk[4]); q2o[1] = 0u;
  }
  TS(5);
  wait_flags(ws + WS_KVFLAG, 256, tag, lane);
  TS(6);
  const __amdgpu_buffer_rsrc_t ri = mk_rs(ws + WS_IMG, 64 * TILE_B);
  const int kblk = 2 * (r32 >> 4) + ((r32 >> 2) & 1), kj = 4 * ((r32 >> 3) & 1) + (r32 & 3);
  const unsigned vk1 = kblk * BLK_B + (kj * 2 + hi) * 16, vk2 = kblk * BLK_B + K2_OFF + kj * 8;
  const unsigned vv = r32 < 19 ? (unsigned)(hi * BLK_B + V_OFF + r32 * 16) : 0x40000000u;
#define LDK1(t) __builtin_amdgcn_raw_buffer_load_b128(ri, vk1, (t) * TILE_B, 16)
#define LDK2(t) __builtin_amdgcn_raw_buffer_load_b64(ri, vk2, (t) * TILE_B, 16)
#define LDV0(t) __builtin_amdgcn_raw_buffer_load_b128(ri, vv, (t) * TILE_B, 16)
#define LDV1(t) __builtin_amdgcn_raw_buffer_load_b128(ri, vv + 2 * BLK_B, (t) * TILE_B, 16)
  const int t0 = ((wid >> 1) & 1) * 32, t1 = t0 + 32;
  u32x4 kA = LDK1(t0), kB = LDK1(t0 + 1); u32x2 k2A = LDK2(t0), k2B = LDK2(t0 + 1);
  u32x4 vA0 = LDV0(t0), vA1 = LDV1(t0);
  {
    f32x16 s = MFMA16(BC8(kA), BC8(q1o), zero), s2 = MFMA16(BC8(kB), BC8(q1o), zero);
    s = MFMA8(BC4(k2A), BC4(q2o), s); s2 = MFMA8(BC4(k2B), BC4(q2o), s2);
    float m = -3.0e38f;
#pragma unroll
    for (int i = 0; i < 16; ++i) m = fmaxf(fmaxf(m, s[i]), s2[i]);
    m = -(swapmax(m) + 16.f);
    const unsigned mh = pk2(m, 0.f) & 0xffffu;
    const unsigned mm = mh | (pk2(0.f, m - bflo(mh)) & 0xffff0000u);
    const unsigned qb = L0 + L_QX + wid * 1536;
    lds_st<u32x4>(qb + lane * 16, q1o); lds_st<u32x2>(qb + 1024 + lane * 8, u32x2{q2o[0], hi ? 0u : mm});
  }
  TS(7);
  BAR();
  TS(8);
  if (wid < 4) {
    u32x4 q1[4]; u32x2 q2[4];
#pragma unroll
    for (int n = 0; n < 4; ++n) { const unsigned qb = L0 + L_QX + (2 * n + qh) * 1536; q1[n] = lds_ld<u32x4>(qb + lane * 16); q2[n] = lds_ld<u32x2>(qb + 1024 + lane * 8); }
    f32x16 o[4];
#pragma unroll
    for (int n = 0; n < 4; ++n) o[n] = zero;
    u32x4 vB0, vB1 = vA1, p1 = {0, 0, 0, 0}; f32x4 e = {0.f, 0.f, 0.f, 0.f};
    f32x16 sa, sb;
    asm volatile("s_waitcnt lgkmcnt(0)" : "+v"(q1[0]), "+v"(q1[1]), "+v"(q1[2]), "+v"(q1[3]), "+v"(q2[0]), "+v"(q2[1]), "+v"(q2[2]), "+v"(q2[3]));
    sa = MFMA16(BC8(kA), BC8(q1[0]), zero); sa = MFMA8(BC4(k2A), BC4(q2[0]), sa);
#pragma unroll 1
    for (int t = t0; t < t1; t += 2) {
      vB0 = LDV0(t + 1); SB();
      step(sb, sa, o[3], o[0], p1, e, kA, k2A, q1[1], q2[1], vB1, vA0, zero);
      vB1 = LDV1(t + 1); SB();
      step(sa, sb, o[0], o[1], p1, e, kA, k2A, q1[2], q2[2], vA1, vA0, zero);
      step(sb, sa, o[1], o[2], p1, e, kA, k2A, q1[3], q2[3], vA1, vA0, zero);
      kA = LDK1(t + 2); k2A = LDK2(t + 2); SB();
      step(sa, sb, o[2], o[3], p1, e, kB, k2B, q1[0], q2[0], vA1, vA0, zero);
      vA0 = LDV0(t + 2); SB();
      step(sb, sa, o[3], o[0], p1, e, kB, k2B, q1[1], q2[1], vA1, vB0, zero);
      vA1 = LDV1(t + 2); SB();
      step(sa, sb, o[0], o[1], p1, e, kB, k2B, q1[2], q2[2], vB1, vB0, zero);
      step(sb, sa, o[1], o[2], p1, e, kB, k2B, q1[3], q2[3], vB1, vB0, zero);
      kB = LDK1(t + 3); k2B = LDK2(t + 3); SB();
      step(sa, sb, o[2], o[3], p1, e, kA, k2A, q1[0], q2[0], vB1, vB0, zero);
    }
    Cv(p1, 2, e);
    o[3] = MFMA16(BC8(vB1), BC8(p1), o[3]);
    TS(9);
    aux_sync(L0 + L_MISC + 4, 8, lane);
#pragma unroll
    for (int n = 0; n < 4; ++n) {
      const unsigned cb = L0 + L_CMB + ((2 * n + qh) * 2 + (wid >> 1)) * 3072 + lane * 16;
      lds_st<f32x4>(cb, f32x4{o[n][0], o[n][1], o[n][2], o[n][3]}); lds_st<f32x4>(cb + 1024, f32x4{o[n][4], o[n][5], o[n][6], o[n][7]}); lds_st<f32x4>(cb + 2048, f32x4{o[n][8], o[n][9], o[n][10], o[n][11]});
    }
  } else {
#define AUX_PRIO 2
    __builtin_amdgcn_s_setprio(AUX_PRIO);
    const int a = wid - 4, head = hg * 4 + a;
    const unsigned cnt = L0 + L_MISC;
    {
      const u32x4 rs_xa = mk_rsrc(A.Ax + ((size_t)rb * 64 - 16) * D, 80 * D * 2), rs_w = mk_rsrc(A.Awqkv, D * 3 * D * 2);
      const unsigned xv = xt_voff(lane, a & 1);
      for (int oc = a; oc < 10; oc += 4) if (oc >= 2 || rb > 0) for (int kc = 0; kc < 9; ++kc) dma16(rs_xa, L0 + L_XA + (kc * 10 + oc) * 1024, xv, oc * 8 * D * 2 + kc * 128);
      const int cb = (head * 36) & ~15, sh = (head * 36) & 8;
      unsigned wv_[5];
#pragma unroll
      for (int i = 0; i < 5; ++i) { const int idx = 64 * i + lane, row = (idx * 7282) >> 16, pc = idx - row * 9, seg = (pc * 11) >> 5;
        wv_[i] = (unsigned)(row * (3 * D * 2) + seg * (D * 2) + cb + (pc - seg * 3) * 16); }
      const unsigned ring = L0 + L_RING + a * RING_B;
#define WISSUE(c, slot) do { const unsigned so_ = (c) * 32 * (3 * D * 2); \
        dma16(rs_w, (slot), wv_[0], so_); dma16(rs_w, (slot) + 1024, wv_[1], so_); dma16(rs_w, (slot) + 2048, wv_[2], so_); dma16(rs_w, (slot) + 3072, wv_[3], so_); dma16_lo(rs_w, (slot) + 4096, wv_[4], so_); } while (0)
      WISSUE(0, ring); WISSUE(1, ring + 4608);
      WAITV(10);
      aux_sync(cnt, 4, lane);
      TS(9);
      unsigned wo_[2];
#pragma unroll
      for (int n = 0; n < 2; ++n) { int gm = 8 * n + 4 * (g4 & 1) + tp; gm = gm > 14 ? 14 : gm; const int sg = gm / 5, gi = gm - sg * 5;
        wo_[n] = (unsigned)((8 * hi + tq) * 144 + (sg == 2 ? 0 : sg + 1) * 48 + sh + gi * 8); }
      unsigned xo[3];
#pragma unroll
      for (int mt = 0; mt < 3; ++mt) { int row = mt * 32 - 16 + r32; row = row < 0 ? 0 : row; xo[mt] = L0 + L_XA + row * 128 + ((hi ^ ((row >> 1) & 7)) << 4); }
      f32x16 acc[3][2];
#pragma unroll
      for (int mt = 0; mt < 3; ++mt) { acc[mt][0] = zero; acc[mt][1] = zero; }
      unsigned scur = ring, snxt = ring + 4608, sfree = ring + 9216;
#pragma unroll 1
      for (int c = 0; c < 18; ++c) {
        WISSUE(c + 2 < 18 ? c + 2 : 17, sfree);
        WAITV(10);
#pragma unroll
        for (int s = 0; s < 2; ++s) {
          const unsigned xk = (c >> 1) * 10240, xs = (((c & 1) * 2 + s) << 5);
          bf16x8 wf[2], xf[3];
#pragma unroll
          for (int n = 0; n < 2; ++n) wf[n] = cat(trd(scur + wo_[n] + s * 2304), trd(scur + wo_[n] + s * 2304 + 576));
#pragma unroll
          for (int mt = 0; mt < 3; ++mt) xf[mt] = lds_ld<bf16x8>((xo[mt] ^ xs) + xk);
#pragma unroll
          for (int mt = 0; mt < 3; ++mt) { acc[mt][0] = MFMA16(wf[0], xf[mt], acc[mt][0]); acc[mt][1] = MFMA16(wf[1], xf[mt], acc[mt][1]); }
        }
        const unsigned t_ = scur; scur = snxt; snxt = sfree; sfree = t_;
        SB();
      }
      WAITV(0);
      aux_sync(cnt, 8, lane);
      TS(10);
      const unsigned fb = L0 + L_F32 + a * F32_B;
#pragma unroll
      for (int mt = 0; mt < 3; ++mt)
#pragma unroll
        for (int n = 0; n < 2; ++n)
#pragma unroll
          for (int g = 0; g < 4; ++g)
            if (!(n == 1 && g == 3) || !hi) lds_st<f32x4>(fb + (mt * 32 + r32) * 240 + (8 * n + 2 * g + hi) * 16, f32x4{acc[mt][n][4 * g], acc[mt][n][4 * g + 1], acc[mt][n][4 * g + 2], acc[mt][n][4 * g + 3]});
    }
    {
      const unsigned rowa = L0 + L_F32 + a * F32_B + (32 + lane) * 240;
      f32x4 q[5];
#pragma unroll
      for (int i = 0; i < 5; ++i) q[i] = lds_ld<f32x4>(rowa + 160 + i * 16);
      if (head & 1) { q[0][0] = 0.f; q[0][1] = 0.f; } else { q[4][2] = 0.f; q[4][3] = 0.f; }
      float lg[8], mx = -3.0e38f;
      const int tok = rb * 64 + lane;
#pragma unroll
      for (int w = 0; w < 8; ++w) {
        f32x4 kv[5];
#pragma unroll
        for (int i = 0; i < 5; ++i) kv[i] = lds_ld<f32x4>(rowa - w * 480 + i * 16);
        float s0 = 0.f, s1 = 0.f;
#pragma unroll
        for (int i = 0; i < 5; ++i) { fmac(s0, q[i][0], kv[i][0]); fmac(s1, q[i][1], kv[i][1]); fmac(s0, q[i][2], kv[i][2]); fmac(s1, q[i][3], kv[i][3]); }
        const float s = (s0 + s1) * QSCALE;
        lg[w] = tok - 2 * w < 0 ? -1.4426950408889634e9f : s;
        mx = fmaxf(mx, lg[w]);
      }
      float l = 0.f; float o[5][4];
#pragma unroll
      for (int i = 0; i < 5; ++i) { o[i][0] = 0.f; o[i][1] = 0.f; o[i][2] = 0.f; o[i][3] = 0.f; }
#pragma unroll
      for (int w = 0; w < 8; ++w) {
        const float p = EX(lg[w] - mx); l += p;
        const unsigned va = tok - 2 * w < 0 ? rowa : rowa - w * 480;
#pragma unroll
        for (int i = 0; i < 5; ++i) { const f32x4 vvv = lds_ld<f32x4>(va + 80 + i * 16); fmac(o[i][0], p, vvv[0]); fmac(o[i][1], p, vvv[1]); fmac(o[i][2], p, vvv[2]); fmac(o[i][3], p, vvv[3]); }
      }
      const float rl = 1.f / l;
      unsigned pk[10];
#pragma unroll
      for (int i = 0; i < 5; ++i) { pk[2 * i] = pk2(o[i][0] * rl, o[i][1] * rl); pk[2 * i + 1] = pk2(o[i][2] * rl, o[i][3] * rl); }
      const unsigned sg = L0 + L_STG_A + (lane >> 4) * 2304 + i16 * 16;
      unsigned odd = (head & 1) ? ~0u : 0u; asm volatile("" : "+v"(odd));
#pragma unroll
      for (int i = 0; i < 9; ++i) { const int c = a * 18 + 2 * i; lds_st<unsigned>(sg + (c >> 3) * 256 + (c & 7) * 2, (pk[i + 1] & odd) | (pk[i] & ~odd)); }
    }
    aux_sync(cnt, 12, lane);
    TS(11);
    publish_o(ws + WS_OA, rb, hg, L0 + L_STG_A, a, 4, lane);
    aux_sync(cnt, 16, lane);
    if (wid == 4 && lane == 0) __hip_atomic_store((GAS unsigned*)(ws + WS_OAFLAG) + g8, tag, __ATOMIC_RELAXED, __HIP_MEMORY_SCOPE_AGENT);
    const unsigned wva = ws_voff(lane, a);
    {
      const u32x4 rs_wo = mk_rsrc(A.Awo + hg * 72, (D * D - hg * 72) * 2);
      for (int kc = 0; kc < 9; ++kc) { dma16(rs_wo, L0 + L_WO + kc * 9216 + a * 1024, wva, kc * 64 * D * 2); dma16(rs_wo, L0 + L_WO + kc * 9216 + wid * 1024, wvw, kc * 64 * D * 2); }
      for (int kc = a; kc < 9; kc += 4) dma16(rs_wo, L0 + L_WO + kc * 9216 + 8 * 1024, wv8, kc * 64 * D * 2);
    }
    wait_flags(ws + WS_OAFLAG + rb * 32, 8, tag, lane);
    TS(12);
    {
      const __amdgpu_buffer_rsrc_t ro = mk_rs(ws + WS_OA + (size_t)rb * 73728 + a * 18432, 18432);
      u32x4 of[18];
#pragma unroll
      for (int ks = 0; ks < 18; ++ks) of[ks] = __builtin_amdgcn_raw_buffer_load_b128(ro, lane * 16 + (ks & 3) * 1024, (ks >> 2) * 4096, 16);
      aux_sync(cnt, 20, lane);
      f32x4 acc[5];
#pragma unroll
      for (int n = 0; n < 5; ++n) acc[n] = f32x4{0.f, 0.f, 0.f, 0.f};
      const unsigned wa = L0 + L_WO + (8 * g4 + tq) * 144 + tp * 8, w4 = tp < 2 ? wa + 128 : wa + 112;
#pragma unroll
      for (int ks = 0; ks < 18; ++ks) {
#pragma unroll
        for (int n = 0; n < 5; ++n) { const unsigned p = (n < 4 ? wa + n * 32 : w4) + ks * 4608; acc[n] = MF16(cat(trd(p), trd(p + 576)), BC8(of[ks]), acc[n]); }
      }
      const unsigned sa = L0 + L_OUT_A + (a * 16 + i16) * 160 + g4 * 8;
#pragma unroll
      for (int n = 0; n < 5; ++n) if (n < 4 || g4 < 2) lds_st<u32x2>(sa + n * 32, u32x2{pk2(acc[n][0], acc[n][1]), pk2(acc[n][2], acc[n][3])});
    }
    aux_sync(cnt, 24, lane);
    store_out(A.out + (size_t)rb * 64 * (2 * D) + hg * 72, L0 + L_OUT_A, tid - 256, 256);
    {
      const u32x4 rs_wo = mk_rsrc(A.Bwo + hg * 72, (D * D - hg * 72) * 2);
      for (int kc = 0; kc < 9; ++kc) { dma16(rs_wo, L0 + L_WO + kc * 9216 + a * 1024, wva, kc * 64 * D * 2); dma16(rs_wo, L0 + L_WO + kc * 9216 + wid * 1024, wvw, kc * 64 * D * 2); }
      for (int kc = a; kc < 9; kc += 4) dma16(rs_wo, L0 + L_WO + kc * 9216 + 8 * 1024, wv8, kc * 64 * D * 2);
    }
    WAITV(0);
    TS(13);
    __builtin_amdgcn_s_setprio(0);
    aux_sync(L0 + L_MISC + 4, 8, lane);
  }
  BAR();
  TS(14);
  if (b == 0 && tid == 0) __hip_atomic_store((GAS unsigned*)(ws + WS_EPOCH), tag, __ATOMIC_RELAXED, __HIP_MEMORY_SCOPE_AGENT);
  {
    float of[12];
#pragma unroll
    for (int c = 0; c < 3; ++c) { const f32x4 u = lds_ld<f32x4>(L0 + L_CMB + wid * 6144 + c * 1024 + lane * 16), v = lds_ld<f32x4>(L0 + L_CMB + wid * 6144 + 3072 + c * 1024 + lane * 16);
      of[4 * c] = u[0] + v[0]; of[4 * c + 1] = u[1] + v[1]; of[4 * c + 2] = u[2] + v[2]; of[4 * c + 3] = u[3] + v[3]; }
    float l = lowhalf(of[10]);
    float asum = 0.f;
#pragma unroll
    for (int i = 0; i < 10; ++i) asum += fabsf(of[i]);
    const bool ok = (l > 3.0e-6f) && (l < 1.0e37f) && (asum < 1.0e37f);
    if (__builtin_amdgcn_readfirstlane(__any(!ok))) {
      float m = -3.0e38f;
#pragma unroll 1
      for (int t = 0; t < 64; ++t) { f32x16 s = MFMA16(BC8(LDK1(t)), BC8(q1o), zero); s = MFMA8(BC4(LDK2(t)), BC4(q2o), s);
#pragma unroll
        for (int i = 0; i < 16; i += 2) m = fmaxf(fmaxf(m, s[i]), s[i + 1]); }
      m = swapmax(m);
      f32x16 os = zero;
#pragma unroll 1
      for (int t = 0; t < 64; ++t) {
        f32x16 s = MFMA16(BC8(LDK1(t)), BC8(q1o), zero); s = MFMA8(BC4(LDK2(t)), BC4(q2o), s);
        u32x4 p0, p1;
#pragma unroll
        for (int i = 0; i < 4; ++i) { p0[i] = pk2(EX(s[2 * i] - m), EX(s[2 * i + 1] - m)); p1[i] = pk2(EX(s[8 + 2 * i] - m), EX(s[9 + 2 * i] - m)); }
        os = MFMA16(BC8(LDV0(t)), BC8(p0), os); os = MFMA16(BC8(LDV1(t)), BC8(p1), os);
      }
#pragma unroll
      for (int i = 0; i < 12; ++i) of[i] = os[i];
      l = lowhalf(of[10]);
    }
    const float rl = 1.f / l;
    const unsigned sg = L0 + L_STG_B + (qh * 2 + (r32 >> 4)) * 2304 + i16 * 16;
#pragma unroll
    for (int i = 0; i < 5; ++i) {
      const int c = hl * 18 + (i >> 1) * 8 + (i & 1) * 2 + 4 * hi;
      if (i < 4 || !hi) lds_st<unsigned>(sg + (c >> 3) * 256 + (c & 7) * 2, pk2(of[2 * i] * rl, of[2 * i + 1] * rl));
    }
  }
  BAR();
  publish_o(ws + WS_OB, rb, hg, L0 + L_STG_B, wid, 8, lane);
  BAR();
  if (tid == 0) __hip_atomic_store((GAS unsigned*)(ws + WS_OBFLAG) + g8, tag, __ATOMIC_RELAXED, __HIP_MEMORY_SCOPE_AGENT);
  TS(15);
  {
    const int mq = wid & 3, kh = wid >> 2;
    const unsigned wa = L0 + L_WO + (kh * 288 + 8 * g4 + tq) * 144 + tp * 8, w4 = tp < 2 ? wa + 128 : wa + 112;
    bf16x8 wf[9][5];
#pragma unroll
    for (int ks = 0; ks < 9; ++ks)
#pragma unroll
      for (int n = 0; n < 5; ++n) { const unsigned p = (n < 4 ? wa + n * 32 : w4) + ks * 4608; wf[ks][n] = cat(trd(p), trd(p + 576)); }
    SB();
    wait_flags(ws + WS_OBFLAG + rb * 32, 8, tag, lane);
    TS(16);
    const __amdgpu_buffer_rsrc_t ro = mk_rs(ws + WS_OB + (size_t)rb * 73728 + mq * 18432 + kh * 9216, 9216);
    u32x4 of[9];
#pragma unroll
    for (int ks = 0; ks < 9; ++ks) of[ks] = __builtin_amdgcn_raw_buffer_load_b128(ro, lane * 16 + (ks & 3) * 1024, (ks >> 2) * 4096, 16);
    f32x4 acc[5];
#pragma unroll
    for (int n = 0; n < 5; ++n) acc[n] = f32x4{0.f, 0.f, 0.f, 0.f};
#pragma unroll
    for (int ks = 0; ks < 9; ++ks)
#pragma unroll
      for (int n = 0; n < 5; ++n) acc[n] = MF16(wf[ks][n], BC8(of[ks]), acc[n]);
    TS(17);
    if (kh) {
#pragma unroll
      for (int n = 0; n < 5; ++n) lds_st<f32x4>(L0 + L_RED + (mq * 5 + n) * 1024 + lane * 16, acc[n]);
    }
    BAR();
    if (!kh) {
      const unsigned sa = L0 + L_OUT_B + (mq * 16 + i16) * 160 + g4 * 8;
#pragma unroll
      for (int n = 0; n < 5; ++n) { const f32x4 v = acc[n] + lds_ld<f32x4>(L0 + L_RED + (mq * 5 + n) * 1024 + lane * 16);
        if (n < 4 || g4 < 2) lds_st<u32x2>(sa + n * 32, u32x2{pk2(v[0], v[1]), pk2(v[2], v[3])}); }
    }
    BAR();
    store_out(A.out + (size_t)rb * 64 * (2 * D) + D + hg * 72, L0 + L_OUT_B, tid, 512);
  }
  TS(18);
}

extern "C" void kernel_launch(void* const* d_in, const int* in_sizes, int n_in, void* d_out, int out_size, void* d_ws, size_t ws_size, hipStream_t stream) {
  Args a;
  a.Ax = (const u16*)d_in[0]; a.Awqkv = (const u16*)d_in[1]; a.Awo = (const u16*)d_in[2];
  a.Bx = (const u16*)d_in[3]; a.Bwq = (const u16*)d_in[4]; a.Bwk = (const u16*)d_in[5]; a.Bwv = (const u16*)d_in[6]; a.Bwo = (const u16*)d_in[7];
  a.ws = (char*)d_ws; a.out = (u16*)d_out;
  hipFuncSetAttribute((const void*)k_fused, hipFuncAttributeMaxDynamicSharedMemorySize, 160 * 1024);
  k_fused<<<256, 512, 160 * 1024, stream>>>(a);
}
```

```cpp
#include <hip/hip_runtime.h>
#include <hip/hip_bf16.h>
#include <stdint.h>

typedef __attribute__((ext_vector_type(8))) __bf16 bf16x8;
typedef __attribute__((ext_vector_type(4))) short s16x4;
typedef __attribute__((ext_vector_type(8))) short s16x8;
typedef __attribute__((ext_vector_type(2))) __bf16 bf16x2;
typedef __attribute__((ext_vector_type(2))) float f32x2;
typedef __attribute__((ext_vector_type(16))) float f32x16;
typedef __attribute__((ext_vector_type(4))) float f32x4;
typedef __attribute__((ext_vector_type(4))) unsigned u32x4;
typedef __attribute__((ext_vector_type(2))) unsigned u32x2;
typedef unsigned short u16;
#define LAS __attribute__((address_space(3)))
#define GAS __attribute__((address_space(1)))
#define DI __device__ __forceinline__
#define SB() __builtin_amdgcn_sched_barrier(0)
#define BC8(x) __builtin_bit_cast(bf16x8, x)
#define BC4(x) __builtin_bit_cast(s16x4, x)
#define MFMA16(a, b, c) __builtin_amdgcn_mfma_f32_32x32x16_bf16(a, b, c, 0, 0, 0)
#define MFMA8(a, b, c) __builtin_amdgcn_mfma_f32_32x32x8bf16_1k(a, b, c, 0, 0, 0)
#define MF16(a, b, c) __builtin_amdgcn_mfma_f32_16x16x32_bf16(a, b, c, 0, 0, 0)
#define EX(x) __builtin_amdgcn_exp2f(x)
#define WAITV_(n) asm volatile("s_waitcnt vmcnt(" #n ")" ::: "memory")
#define WAITV(n) WAITV_(n)
#define BAR() asm volatile("s_waitcnt lgkmcnt(0)\n\ts_barrier" ::: "memory")
#define PRIO(n) do { SB(); __builtin_amdgcn_s_setprio(n); SB(); } while (0)
#define PIN(x) asm volatile("" : "+v"(x))
#define TS(i)
#define PACE 3

constexpr int S = 2048, D = 576, DH = 18, RB = D * 2;
constexpr float QSCALE = 0.23570226039551584f * 1.4426950408889634f;
constexpr int BLK_B = 640, TILE_B = 4 * BLK_B, K2_OFF = 256, V_OFF = 320;
constexpr size_t WS_EPOCH = 0, WS_KVFLAG = 1024, WS_OAFLAG = 2048, WS_OBFLAG = 3072, WS_IMG = 4096, WS_OA = 262144, WS_OB = WS_OA + (size_t)S * RB;
constexpr int SPIN_MAX = 1 << 17;
constexpr unsigned OOB = 0x40000000u;
constexpr int L_R0 = 0, L_XB = 46080, L_WQ0 = 119808, L_D = 156672, L_MISC = 161792, WV_OFF = 21504;
constexpr int L_QX = 151552, QX_B = 1280;
constexpr int SLOT_B = 37888, SLOT_X = 27648, F32_S = 912, F32_T = 32 * F32_S;
constexpr int L_WO = 0, L_STG_A = 92160, L_RED_A = 101376;
constexpr int L_CMB = 83968, L_STG_B = 133120, L_RED = 83968;

DI unsigned pk2(float a, float b) { f32x2 v = {a, b}; bf16x2 r = __builtin_convertvector(v, bf16x2); return __builtin_bit_cast(unsigned, r); }
DI float bflo(unsigned u) { return __uint_as_float(u << 16); }
DI s16x4 trd(unsigned a) { return __builtin_amdgcn_ds_read_tr16_b64_v4i16((LAS s16x4*)(uintptr_t)a); }
DI bf16x8 cat(s16x4 a, s16x4 b) { s16x8 v = __builtin_shufflevector(a, b, 0, 1, 2, 3, 4, 5, 6, 7); return BC8(v); }
DI bf16x8 trd2(unsigned a, int second) { return cat(trd(a), trd(a + second)); }
template <class T> DI T lds_ld(unsigned a) { return *(const LAS T*)(uintptr_t)a; }
template <class T> DI void lds_st(unsigned a, T v) { *(LAS T*)(uintptr_t)a = v; }
DI u32x4 mk_rsrc(const void* p, unsigned bytes) { unsigned long long a = (unsigned long long)p; return u32x4{(unsigned)a, (unsigned)(a >> 32) & 0xffffu, bytes, 0x00020000u}; }
DI __amdgpu_buffer_rsrc_t mk_rs(const void* p, unsigned bytes) { return __builtin_amdgcn_make_buffer_rsrc((void*)p, 0, bytes, 0x00020000); }
DI void dma16(u32x4 rs, unsigned lds, unsigned voff, unsigned soff) {
  asm volatile("s_mov_b32 m0, %0\n\ts_nop 0\n\tbuffer_load_dwordx4 %1, %2, %3 offen lds" :: "s"(lds), "v"(voff), "s"(rs), "s"(soff) : "memory");
}
DI float swapmax(float m) { auto rr = __builtin_amdgcn_permlane32_swap(__float_as_uint(m), __float_as_uint(m), false, false); return fmaxf(__uint_as_float(rr[0]), __uint_as_float(rr[1])); }
DI float lowhalf(float m) { auto rr = __builtin_amdgcn_permlane32_swap(__float_as_uint(m), __float_as_uint(m), false, false); return __uint_as_float(rr[0]); }
DI unsigned xt_voff(int lane, int octpar) { return (unsigned)((lane >> 3) * RB + (((lane & 7) ^ ((4 * octpar + (lane >> 4)) & 7)) << 4)); }
DI unsigned ws_voff(int lane, int i, int ld) { const int idx = 64 * i + lane, row = (idx * 7282) >> 16; return (unsigned)(row * (ld - 144) + idx * 16); }
DI void wait_flags(const char* flags, int n, unsigned tag, int lane) {
  const __amdgpu_buffer_rsrc_t rf = mk_rs(flags, n * 4);
  const bool live = lane * 4 < n;
#pragma unroll 1
  for (int spin = 0; spin < SPIN_MAX; ++spin) {
    u32x4 f = __builtin_amdgcn_raw_buffer_load_b128(rf, lane * 16, 0, 16);
    if (__all(!live || (f[0] == tag && f[1] == tag && f[2] == tag && f[3] == tag))) break;
    __builtin_amdgcn_s_sleep(1);
  }
  asm volatile("" ::: "memory");
}
template <int SLP> DI void lsync(unsigned va, unsigned one, unsigned target) {
  unsigned tv, ts, tc;
  asm volatile("s_waitcnt lgkmcnt(0)\n\ts_mov_b64 exec, 1\n\tds_add_u32 %3, %4\n\ts_mov_b64 exec, -1\n\ts_mov_b32 %2, 0x100000\n"
               "1:\n\tds_read_b32 %0, %3\n\ts_waitcnt lgkmcnt(0)\n\tv_readfirstlane_b32 %1, %0\n\ts_nop 0\n\ts_cmp_ge_u32 %1, %5\n\ts_cbranch_scc1 2f\n\t"
               "s_sleep %6\n\ts_sub_u32 %2, %2, 1\n\ts_cbranch_scc0 1b\n2:"
               : "=&v"(tv), "=&s"(ts), "=&s"(tc) : "v"(va), "v"(one), "s"(target), "i"(SLP) : "memory", "scc");
}
DI void fmac(float& acc, float a, float b) { asm("v_fmac_f32 %0, %1, %2" : "+v"(acc) : "v"(a), "v"(b)); }
DI float fmul(float a, float b) { float r; asm("v_mul_f32 %0, %1, %2" : "=v"(r) : "v"(a), "v"(b)); return r; }

DI void Cv(u32x4& p, int i, const f32x4& e) { unsigned x_ = pk2(e[0], e[1]), y_ = pk2(e[2], e[3]); asm volatile("" : "+v"(x_), "+v"(y_)); p[i] = x_; p[i + 1] = y_; SB(); }
DI void Ev(f32x4& e, const f32x16& s, int j) { e[0] = EX(s[j]); e[1] = EX(s[j + 1]); e[2] = EX(s[j + 2]); e[3] = EX(s[j + 3]); asm volatile("" : "+v"(e)); SB(); }
#define STEP(sn, sc, oprev, ocur, kf, k2, qi, v1prev, v0, X0, X2, X3) do { u32x4 p0; \
  sn = MFMA16(BC8(kf), BC8(q1[qi]), zero); SB(); X0; SB(); \
  Cv(p1, 2, e); Ev(e, sc, 0); \
  sn = MFMA8(BC4(k2), BC4(q2[qi]), sn); SB(); \
  Cv(p0, 0, e); Ev(e, sc, 4); \
  oprev = MFMA16(BC8(v1prev), BC8(p1), oprev); SB(); X2; SB(); \
  Cv(p0, 2, e); Ev(e, sc, 8); \
  ocur = MFMA16(BC8(v0), BC8(p0), ocur); SB(); X3; SB(); \
  Cv(p1, 0, e); Ev(e, sc, 12); } while (0)

DI void publish_piece(__amdgpu_buffer_rsrc_t ro, unsigned stg, int i, int lane) {
  const int idx = 64 * i + lane, m16 = (idx * 3641) >> 19;
  __builtin_amdgcn_raw_buffer_store_b128(lds_ld<u32x4>(stg + idx * 16), ro, (unsigned)(m16 * (18432 - 2304) + idx * 16), 0, 16);
}
DI void load_wo(const u16* w, int hg, int koff, int a, unsigned w0, unsigned w1, unsigned w8) {
  const u32x4 rs = mk_rsrc(w + hg * 72, (D * D - hg * 72) * 2);
  int kc = koff;
#pragma unroll 1
  for (int p = 0; p < 9; ++p) {
    dma16(rs, L_WO + kc * 9216 + a * 1024, w0, kc * 64 * RB); WAITV(PACE); dma16(rs, L_WO + kc * 9216 + (a + 4) * 1024, w1, kc * 64 * RB); WAITV(PACE);
    if ((p & 3) == a) dma16(rs, L_WO + kc * 9216 + 8192, w8, kc * 64 * RB);
    kc = kc == 8 ? 0 : kc + 1;
  }
}

struct Args { const u16* Ax; const u16* Awqkv; const u16* Awo; const u16* Bx; const u16* Bwq; const u16* Bwk; const u16* Bwv; const u16* Bwo; char* ws; u16* out; };

__global__ __launch_bounds__(512, 2) void k_fused(Args A) {
  extern __shared__ __attribute__((aligned(16))) char smem[];
  asm volatile("" :: "v"((LAS char*)smem));
  char* const ws = A.ws;
  const int tid = threadIdx.x, lane = tid & 63, wid = __builtin_amdgcn_readfirstlane(tid >> 6), r32 = lane & 31, hi = lane >> 5;
  const int g4 = lane >> 4, i16 = lane & 15, tq = i16 >> 2, tp = i16 & 3;
  const int b = blockIdx.x, xcd = b & 7, jj = b >> 3, rb = xcd * 4 + (jj >> 3), hg = jj & 7, g8 = rb * 8 + hg;
  TS(0);
  { const u32x4 rs_e = mk_rsrc(ws + WS_EPOCH, 4); const unsigned z = 0;
    asm volatile("s_mov_b32 m0, %0\n\ts_mov_b64 exec, 1\n\tbuffer_load_dword %1, %2, 0 offen lds\n\ts_mov_b64 exec, -1" :: "s"(L_MISC + 64 + wid * 4), "v"(z), "s"(rs_e) : "memory"); }
  unsigned tag;
  const u32x4 rs_x = mk_rsrc(A.Bx + (size_t)rb * 64 * D, 64 * RB), rs_wq = mk_rsrc(A.Bwq + hg * 72, (D * D - hg * 72) * 2);
  const int koff = (hg + 2 * (rb & 3)) % 9;
#define KC(p) ((p) + koff >= 9 ? (p) + koff - 9 : (p) + koff)
  unsigned one = 1u, cnt = L_MISC + (wid < 5 ? 12 : 0); PIN(one); PIN(cnt);
  if (tid < 16) lds_st<unsigned>(L_MISC + tid * 4, 0u);
  f32x16 zero;
#pragma unroll
  for (int i = 0; i < 16; ++i) zero[i] = 0.f;
  if (wid < 5) {
    const u32x4 rs_wk = mk_rsrc(A.Bwk, D * DH * 2), rs_wv = mk_rsrc(A.Bwv, D * DH * 2);
    const unsigned xvh = xt_voff(lane, hg & 1) + hg * 8 * RB;
#pragma unroll 1
    for (int p = wid; p < 9; p += 5) dma16(rs_x, L_XB + (p * 8 + hg) * 1024, xvh, KC(p) * 128);
    int ii = (wid + g8) % 21;
#pragma unroll 1
    for (int i = wid; i < 21; i += 5) { dma16(rs_wk, L_R0 + ii * 1024, lane * 16, ii * 1024); dma16(rs_wv, L_R0 + WV_OFF + ii * 1024, lane * 16, ii * 1024); ii = ii >= 16 ? ii - 16 : ii + 5; }
    BAR();
    int G = 4 * wid + tp; G = G > 17 ? 17 : G;
    unsigned wa = L_R0 + (8 * g4 + tq) * 72 + G * 8 + (G >= 9 ? WV_OFF - 72 : 0);
    const int row = hg * 8 + (i16 & 7);
    unsigned xa = L_XB + row * 128 + (((2 * g4) ^ ((row >> 1) & 7)) << 4) - koff * 8192;
    unsigned sel = (i16 & 8) ? 0x07060302u : 0x05040100u;
    unsigned xs[9];
#pragma unroll
    for (int kc = 0; kc < 9; ++kc) { xs[kc] = xa + (kc < koff ? kc + 9 : kc) * 8192; PIN(xs[kc]); }
    PIN(wa); PIN(sel);
    WAITV(0);
    lsync<0>(cnt, one, 5);
    TS(1);
    u32x4 x0[9], x1[9]; bf16x8 wf[9];
#pragma unroll
    for (int kc = 0; kc < 9; ++kc) { x0[kc] = lds_ld<u32x4>(xs[kc]); x1[kc] = lds_ld<u32x4>(xs[kc] ^ 16); wf[kc] = trd2(wa + kc * 2304, 288); }
    tag = lds_ld<unsigned>(L_MISC + 64 + wid * 4) + 1u;
    SB();
    f32x4 d0 = {0.f, 0.f, 0.f, 0.f}, d1 = d0;
#pragma unroll
    for (int kc = 0; kc < 9; ++kc) {
      u32x4 xf = {__builtin_amdgcn_perm(x0[kc][1], x0[kc][0], sel), __builtin_amdgcn_perm(x0[kc][3], x0[kc][2], sel), __builtin_amdgcn_perm(x1[kc][1], x1[kc][0], sel), __builtin_amdgcn_perm(x1[kc][3], x1[kc][2], sel)};
      if (kc & 1) d1 = MF16(wf[kc], BC8(xf), d1); else d0 = MF16(wf[kc], BC8(xf), d0);
    }
    lds_st<f32x4>(L_D + (i16 * 80 + 16 * wid + 4 * g4) * 4, d0 + d1);
    lsync<0>(cnt, one, 10);
    TS(2);
    if (wid == 0) {
      const bool k2c = (lane & 60) == 16;
      const int d = lane > 37 ? 17 : lane - 20;
      const int a0 = lane < 16 ? (lane >> 1) * 80 + (lane & 1) * 10 : (k2c ? (lane - 16) * 160 + 8 : 36 + d), st = lane < 20 ? 4 : 320;
      const unsigned da = L_D + a0 * 4, db = da + 4 * st + (k2c ? 304 : 0);
      float v[8];
#pragma unroll
      for (int e = 0; e < 4; ++e) { v[e] = lds_ld<float>(da + e * st) + lds_ld<float>(da + e * st + 2632); v[4 + e] = lds_ld<float>(db + e * st) + lds_ld<float>(db + e * st + 2632); }
      u32x4 c = {pk2(v[0], v[1]), pk2(v[2], v[3]), pk2(v[4], v[5]), pk2(v[6], v[7])};
      if (k2c || lane == 38) { c[1] = 0x3f803f80u; c[3] = 0x3f803f80u; }
      if (lane == 38) { c[0] = 0x3f803f80u; c[2] = 0x3f803f80u; }
      __builtin_amdgcn_raw_buffer_store_b128(c, mk_rs(ws + WS_IMG + (size_t)g8 * BLK_B, BLK_B), lane * 16, 0, 16);
    } else {
      const unsigned w0 = ws_voff(lane, 2 * wid - 2, RB), w1 = ws_voff(lane, 2 * wid - 1, RB), w8 = ws_voff(lane, 8, RB);
      dma16(rs_wq, L_R0 + (wid - 1) * 9216 + 8192, w8, KC(wid + 3) * 64 * RB);
      dma16(rs_wq, L_R0 + 4 * 9216 + 8192, w8, KC(8) * 64 * RB);
      int kc = KC(4);
#pragma unroll 1
      for (int p = 0; p < 5; ++p) { dma16(rs_wq, L_R0 + p * 9216 + (2 * wid - 2) * 1024, w0, kc * 64 * RB); dma16(rs_wq, L_R0 + p * 9216 + (2 * wid - 1) * 1024, w1, kc * 64 * RB); kc = kc == 8 ? 0 : kc + 1; }
    }
    TS(3);
  } else {
    const int l = wid - 5;
    const unsigned w0 = ws_voff(lane, 3 * l, RB), w1 = ws_voff(lane, 3 * l + 1, RB), w2 = ws_voff(lane, 3 * l + 2, RB);
    const int o0 = (hg + 1 + l) & 7, o1 = (hg + 4 + l) & 7, o2 = (hg + 7) & 7;
    const unsigned x0 = xt_voff(lane, o0 & 1) + o0 * 8 * RB, x1 = xt_voff(lane, o1 & 1) + o1 * 8 * RB, x2 = xt_voff(lane, o2 & 1) + o2 * 8 * RB;
    BAR();
    int kc = koff, t3 = l;
#pragma unroll 1
    for (int p = 0; p < 9; ++p) {
      dma16(rs_x, L_XB + (p * 8 + o0) * 1024, x0, kc * 128); dma16(rs_x, L_XB + (p * 8 + o1) * 1024, x1, kc * 128);
      if (t3 == 0) { dma16(rs_x, L_XB + (p * 8 + o2) * 1024, x2, kc * 128); t3 = 3; }
      --t3;
      if (p < 4) { const unsigned wd = L_WQ0 + p * 9216 + 3 * l * 1024; dma16(rs_wq, wd, w0, kc * 64 * RB); dma16(rs_wq, wd + 1024, w1, kc * 64 * RB); dma16(rs_wq, wd + 2048, w2, kc * 64 * RB); }
      kc = kc == 8 ? 0 : kc + 1;
    }
    TS(3);
    WAITV(22);
    tag = lds_ld<unsigned>(L_MISC + 64 + wid * 4) + 1u;
  }
  const int hl = wid >> 1, qh = wid & 1, par = hl & 1;
  const u32x4 rs_i = mk_rsrc(ws + WS_IMG, 64 * TILE_B);
  const int kblk = 2 * (r32 >> 4) + ((r32 >> 2) & 1), kj = 4 * ((r32 >> 3) & 1) + (r32 & 3);
  const unsigned vk1 = kblk * BLK_B + (kj * 2 + hi) * 16, vk2 = kblk * BLK_B + K2_OFF + kj * 8;
  const unsigned vv = r32 < 19 ? (unsigned)(hi * BLK_B + V_OFF + r32 * 16) : OOB;
#define SC1 " sc1"
#define LD4(dst, voff, so, imm) asm volatile("buffer_load_dwordx4 %0, %1, %2, %3 offen offset:" #imm SC1 : "=v"(dst) : "v"(voff), "s"(rs_i), "s"(so) : "memory")
#define LD2(dst, voff, so, imm) asm volatile("buffer_load_dwordx2 %0, %1, %2, %3 offen offset:" #imm SC1 : "=v"(dst) : "v"(voff), "s"(rs_i), "s"(so) : "memory")
  const unsigned sob = ((wid >> 1) & 1) * 32 * TILE_B, soe = sob + 32 * TILE_B;
  const unsigned so0 = sob + (jj & 15) * 2 * TILE_B;
  u32x4 kA, kB, vA0, vA1; u32x2 k2A, k2B;
  u32x4 q1o; u32x2 q2o;
  {
    f32x16 qa = zero;
    const int gg = 2 * (g4 & 1) + (tp >> 1), hh = tp & 1;
    int col = ((hl * 18) & ~3) + (hh ? (par ? 12 : 8) : 0) + 4 * gg; col = col > 68 ? 68 : col;
    unsigned wb = L_WQ0 + (8 * hi + tq) * 144 + col * 2;
    const int row = qh * 32 + r32;
    unsigned xo = L_XB + row * 128 + ((hi ^ ((row >> 1) & 7)) << 4);
#define QCHUNK() do { bf16x8 wf[4], xf[4]; \
      _Pragma("unroll") for (int s = 0; s < 4; ++s) { wf[s] = trd2(wb + s * 2304, 576); xf[s] = lds_ld<bf16x8>(xo ^ (s << 5)); } \
      SB(); \
      _Pragma("unroll") for (int s = 0; s < 4; ++s) qa = MFMA16(wf[s], xf[s], qa); \
      wb += 9216; xo += 8192; } while (0)
#pragma unroll 1
    for (int p = 0; p < 7; ++p) {
      if (p == 0) { BAR(); TS(4); }
      else if (p == 2) {
        if (wid >= 5) WAITV(11);
        if (wid == 0) { WAITV(0); if (lane == 0) __hip_atomic_store((GAS unsigned*)(ws + WS_KVFLAG) + g8, tag, __ATOMIC_RELAXED, __HIP_MEMORY_SCOPE_AGENT); }
        BAR();
      } else if (p == 4) { WAITV(4); BAR(); wb -= L_WQ0 + 4 * 9216 - L_R0; }
      QCHUNK();
    }
    TS(5);
    wait_flags(ws + WS_KVFLAG, 256, tag, lane);
    LD4(kA, vk1, so0, 0); LD2(k2A, vk2, so0, 0); LD4(kB, vk1, so0, 2560); LD2(k2B, vk2, so0, 2560); LD4(vA0, vv, so0, 0); LD4(vA1, vv, so0, 1280);
    BAR();
    TS(6);
    QCHUNK(); QCHUNK();
    unsigned pk[6];
#pragma unroll
    for (int i = 0; i < 6; ++i) pk[i] = pk2(qa[2 * i] * QSCALE, qa[2 * i + 1] * QSCALE);
    const bool o1 = (par ^ hi) != 0;
#pragma unroll
    for (int i = 0; i < 4; ++i) q1o[i] = o1 ? pk[i + 1] : pk[i];
    q2o[0] = hi ? 0u : (o1 ? pk[5] : pk[4]); q2o[1] = 0u;
  }
  asm volatile("s_waitcnt vmcnt(0)" : "+v"(kA), "+v"(k2A), "+v"(kB), "+v"(k2B), "+v"(vA0), "+v"(vA1) :: "memory");
  {
    f32x16 s = MFMA16(BC8(kA), BC8(q1o), zero), s2 = MFMA16(BC8(kB), BC8(q1o), zero);
    s = MFMA8(BC4(k2A), BC4(q2o), s); s2 = MFMA8(BC4(k2B), BC4(q2o), s2);
    const unsigned qb = L_QX + wid * QX_B;
    lds_st<u32x4>(qb + lane * 16, q1o);
    float m = fmaxf(s[0], s2[0]);
#pragma unroll
    for (int i = 1; i < 16; ++i) m = fmaxf(fmaxf(m, s[i]), s2[i]);
    m = -(swapmax(m) + 16.f);
    const unsigned mh = pk2(m, 0.f) & 0xffffu;
    const unsigned mm = mh | (pk2(0.f, m - bflo(mh)) & 0xffff0000u);
    if (!hi) lds_st<u32x2>(qb + 1024 + lane * 8, u32x2{q2o[0], mm});
  }
  TS(7);
  BAR();
  TS(8);
  cnt = L_MISC; PIN(cnt);
  unsigned cntj = L_MISC + 4; PIN(cntj);
  if (wid < 4) {
    u32x4 q1[4]; u32x2 q2[4];
    const unsigned q2a = hi ? OOB : (unsigned)(L_QX + qh * QX_B + 1024 + r32 * 8);
#pragma unroll
    for (int n = 0; n < 4; ++n) { q1[n] = lds_ld<u32x4>(L_QX + (2 * n + qh) * QX_B + lane * 16); q2[n] = lds_ld<u32x2>(q2a + 2 * n * QX_B); }
    u32x4 vB0, vB1 = vA1, p1 = {0, 0, 0, 0}; f32x4 e = {0.f, 0.f, 0.f, 0.f};
    f32x16 acc[4];
#pragma unroll
    for (int n = 0; n < 4; ++n) { PIN(p1); acc[n] = MFMA16(BC8(p1), BC8(p1), zero); }
    f32x16 sa, sb;
    asm volatile("s_waitcnt lgkmcnt(0)" : "+v"(q1[0]), "+v"(q1[1]), "+v"(q1[2]), "+v"(q1[3]), "+v"(q2[0]), "+v"(q2[1]), "+v"(q2[2]), "+v"(q2[3]));
    sa = MFMA16(BC8(kA), BC8(q1[0]), zero); sa = MFMA8(BC4(k2A), BC4(q2[0]), sa);
    unsigned sc = so0, sn = so0 + 2 * TILE_B; sn = sn == soe ? sob : sn;
#define NOP_ do {} while (0)
#pragma unroll 1
    for (int it = 0; it < 16; ++it) {
      STEP(sb, sa, acc[3], acc[0], kA, k2A, 1, vB1, vA0, LD4(vB0, vv, sc, 2560), asm volatile("s_waitcnt vmcnt(3)" : "+v"(vA0), "+v"(vA1)), NOP_);
      STEP(sa, sb, acc[0], acc[1], kA, k2A, 2, vA1, vA0, LD4(vB1, vv, sc, 3840), asm volatile("s_mov_b32 %0, %1" : "=s"(sc) : "s"(sn)), asm volatile("s_add_u32 %0, %0, 0x1400" : "+s"(sn) :: "scc"));
      STEP(sb, sa, acc[1], acc[2], kA, k2A, 3, vA1, vA0, asm volatile("s_cmp_eq_u32 %0, %1\n\ts_cselect_b32 %0, %2, %0" : "+s"(sn) : "s"(soe), "s"(sob) : "scc"), NOP_, asm volatile("s_waitcnt vmcnt(2)" : "+v"(kB), "+v"(k2B)));
      STEP(sa, sb, acc[2], acc[3], kB, k2B, 0, vA1, vA0, LD4(kA, vk1, sc, 0), LD2(k2A, vk2, sc, 0), NOP_);
      STEP(sb, sa, acc[3], acc[0], kB, k2B, 1, vA1, vB0, LD4(vA0, vv, sc, 0), asm volatile("s_waitcnt vmcnt(3)" : "+v"(vB0), "+v"(vB1)), NOP_);
      STEP(sa, sb, acc[0], acc[1], kB, k2B, 2, vB1, vB0, LD4(vA1, vv, sc, 1280), NOP_, NOP_);
      STEP(sb, sa, acc[1], acc[2], kB, k2B, 3, vB1, vB0, NOP_, NOP_, asm volatile("s_waitcnt vmcnt(2)" : "+v"(kA), "+v"(k2A)));
      STEP(sa, sb, acc[2], acc[3], kA, k2A, 0, vB1, vB0, LD4(kB, vk1, sc, 2560), LD2(k2B, vk2, sc, 2560), NOP_);
    }
    Cv(p1, 2, e);
    acc[3] = MFMA16(BC8(vB1), BC8(p1), acc[3]);
    asm volatile("s_waitcnt vmcnt(0)" : "+v"(kB), "+v"(k2B), "+v"(vA0), "+v"(vA1));
    TS(9);
    lsync<1>(cntj, one, 8);
    const unsigned cb = L_CMB + (qh * 2 + (wid >> 1)) * 3072 + lane * 16;
#pragma unroll
    for (int n = 0; n < 4; ++n)
#pragma unroll
      for (int c = 0; c < 3; ++c) lds_st<f32x4>(cb + n * 12288 + c * 1024, f32x4{acc[n][4 * c], acc[n][4 * c + 1], acc[n][4 * c + 2], acc[n][4 * c + 3]});
  } else {
    PRIO(1);
    const int a = wid - 4;
    f32x16 acc[5];
    { u32x4 z4 = {0, 0, 0, 0};
#pragma unroll
      for (int n = 0; n < 5; ++n) { PIN(z4); acc[n] = MFMA16(BC8(z4), BC8(z4), zero); } }
    {
      const u32x4 rs_xa = mk_rsrc(A.Ax + ((size_t)rb * 64 - 16) * D, 80 * RB), rs_w = mk_rsrc(A.Awqkv + hg * 72, (D * 3 * D - hg * 72) * 2);
      unsigned w0 = ws_voff(lane, a, 3 * RB), w1 = ws_voff(lane, a + 4, 3 * RB), w8 = ws_voff(lane, 8, 3 * RB), xv = xt_voff(lane, a & 1);
      const unsigned xs0 = (rb == 0 && a < 2) ? OOB : a * 8 * RB;
      const int oc2 = a < 2 ? a + 8 : a + 4, s8 = a < 3 ? a : 2;
#define APIECE(u, kc, slot) do { \
        if ((u) == 0) dma16(rs_xa, (slot) + SLOT_X + a * 1024, xv, xs0 + (kc) * 128); \
        else if ((u) == 1) dma16(rs_xa, (slot) + SLOT_X + (a + 4) * 1024, xv, (a + 4) * 8 * RB + (kc) * 128); \
        else if ((u) == 2) dma16(rs_xa, (slot) + SLOT_X + oc2 * 1024, xv, oc2 * 8 * RB + (kc) * 128); \
        else if ((u) == 9) dma16(rs_w, (slot) + s8 * 9216 + 8192, w8, (kc) * 64 * 3 * RB + s8 * RB); \
        else dma16(rs_w, (slot) + (((u) - 3) >> 1) * 9216 + (a + 4 * (((u) - 3) & 1)) * 1024, (((u) - 3) & 1) ? w1 : w0, (kc) * 64 * 3 * RB + (((u) - 3) >> 1) * RB); } while (0)
      const int gl = 4 * (g4 & 1) + tp, ty = a < 2 ? 5 + a : 4;
      unsigned wa[2], xa[4], xb[4];
#pragma unroll
      for (int n = 0; n < 2; ++n) { int gm = 8 * (n ? ty : a) + gl; gm = gm > 53 ? 53 : gm; const int sg = (gm * 57) >> 10;
        wa[n] = (unsigned)((8 * hi + tq) * 144 + (sg == 2 ? 0 : sg + 1) * 9216 + (gm - sg * 18) * 8); PIN(wa[n]); }
#pragma unroll
      for (int s = 0; s < 4; ++s) { const unsigned t_ = (unsigned)(SLOT_X + (r32 - 16) * 128 + (((2 * s + hi) ^ ((r32 >> 1) & 7)) << 4)); xa[s] = t_ + (a == 3 ? 8192 : 0); xb[s] = t_ + (a == 3 ? 0 : 4096); PIN(xa[s]); PIN(xb[s]); }
      PIN(w0); PIN(w1); PIN(w8); PIN(xv);
      PRIO(0);
      int kn = koff;
#pragma unroll 1
      for (int c = 0; c < 3; ++c) {
#pragma unroll
        for (int u = 0; u < 10; ++u) { APIECE(u, kn, c * SLOT_B); WAITV(PACE); }
        kn = kn == 8 ? 0 : kn + 1;
      }
#pragma unroll 1
      for (int c = 0; c < 9; ++c) {
        if (c < 7) WAITV(20); else if (c == 7) WAITV(10); else WAITV(0);
        PRIO(1);
        lsync<1>(cnt, one, 4 * (c + 1));
        PRIO(0);
        if (c == 0) TS(9);
        const unsigned sn = ((c + 3) & 3) * SLOT_B;
        bf16x8 wf[2][2], xf[2][3];
#define AREAD(s, bf) do { \
          wf[bf][0] = trd2(wa[0] + (s) * 2304, 576); xf[bf][0] = lds_ld<bf16x8>(xa[s]); xf[bf][1] = lds_ld<bf16x8>(xb[s]); xf[bf][2] = lds_ld<bf16x8>(xb[s] + 4096); \
          wf[bf][1] = trd2(wa[1] + (s) * 2304, 576); SB(); } while (0)
#define AP(u) do { if (c < 6) APIECE(u, kn, sn); SB(); } while (0)
#define AMMA(bf, u0, u1, u2) do { \
          acc[0] = MFMA16(wf[bf][0], xf[bf][0], acc[0]); SB(); AP(u0); acc[1] = MFMA16(wf[bf][0], xf[bf][1], acc[1]); acc[2] = MFMA16(wf[bf][0], xf[bf][2], acc[2]); SB(); AP(u1); \
          acc[3] = MFMA16(wf[bf][1], xf[bf][1], acc[3]); acc[4] = MFMA16(wf[bf][1], xf[bf][2], acc[4]); \
          SB(); if (u2 >= 0) AP(u2 < 0 ? 0 : u2); } while (0)
        AREAD(0, 0); AREAD(1, 1); AMMA(0, 0, 1, 2); AREAD(2, 0); AMMA(1, 3, 4, -1); AREAD(3, 1);
        { const int dl = (c & 3) == 3 ? -3 * SLOT_B : SLOT_B;
          PRIO(1); wa[0] += dl; wa[1] += dl; PIN(wa[0]); PIN(wa[1]);
          _Pragma("unroll") for (int s = 0; s < 4; ++s) { xa[s] += dl; xb[s] += dl; PIN(xa[s]); PIN(xb[s]); }
          PRIO(0); }
        AMMA(0, 5, 6, 7); AMMA(1, 8, 9, -1);
        kn = kn == 8 ? 0 : kn + 1;
      }
      PRIO(1);
      lsync<1>(cnt, one, 40);
      TS(10);
      const unsigned f0 = r32 * F32_S + (8 * a + hi) * 16 + (a == 3 ? 2 * F32_T : 0), f1 = r32 * F32_S + (8 * a + hi) * 16 + (a == 3 ? 0 : F32_T), fy = f1 + (ty - a) * 128;
#pragma unroll
      for (int j = 0; j < 5; ++j)
#pragma unroll
        for (int g = 0; g < 4; ++g)
          lds_st<f32x4>((j == 0 ? f0 : (j < 3 ? f1 + (j - 1) * F32_T : fy + (j - 3) * F32_T)) + g * 32, f32x4{acc[j][4 * g], acc[j][4 * g + 1], acc[j][4 * g + 2], acc[j][4 * g + 3]});
      lsync<1>(cnt, one, 44);
    }
    {
      const unsigned rowa = (18 + lane) * F32_S + ((a * 72) & ~15);
      f32x4 q[5], kv[8][5];
#pragma unroll
      for (int i = 0; i < 5; ++i) q[i] = lds_ld<f32x4>(rowa + 14 * F32_S + 576 + i * 16);
#pragma unroll
      for (int w = 0; w < 8; ++w)
#pragma unroll
        for (int i = 0; i < 5; ++i) kv[w][i] = lds_ld<f32x4>(rowa + (14 - 2 * w) * F32_S + i * 16);
      SB();
      if (a & 1) { q[0][0] = 0.f; q[0][1] = 0.f; } else { q[4][2] = 0.f; q[4][3] = 0.f; }
      float lg[8], mx;
      const int tok = rb * 64 + lane;
#pragma unroll
      for (int w = 0; w < 8; ++w) {
        float s0 = fmul(q[0][0], kv[w][0][0]), s1 = fmul(q[0][1], kv[w][0][1]);
        fmac(s0, q[0][2], kv[w][0][2]); fmac(s1, q[0][3], kv[w][0][3]);
#pragma unroll
        for (int i = 1; i < 5; ++i) { fmac(s0, q[i][0], kv[w][i][0]); fmac(s1, q[i][1], kv[w][i][1]); fmac(s0, q[i][2], kv[w][i][2]); fmac(s1, q[i][3], kv[w][i][3]); }
        SB();
#pragma unroll
        for (int i = 0; i < 5; ++i) kv[w][i] = lds_ld<f32x4>(rowa + (14 - 2 * w) * F32_S + 288 + i * 16);
        SB();
        const float s = s0 + s1;
        lg[w] = (w > 0 && tok < 2 * w) ? -4.242640687e9f : s;
        mx = w ? fmaxf(mx, lg[w]) : lg[0];
      }
      float l = 0.f; float o[5][4];
      const float nm = -mx * QSCALE;
#pragma unroll
      for (int w = 0; w < 8; ++w) { lg[w] = EX(__builtin_fmaf(lg[w], QSCALE, nm)); l += lg[w]; }
      const float rl = __builtin_amdgcn_rcpf(l);
#pragma unroll
      for (int w = 0; w < 8; ++w) lg[w] = lg[w] * rl;
      asm volatile("s_nop 1" : "+v"(lg[0]), "+v"(lg[1]), "+v"(lg[2]), "+v"(lg[3]), "+v"(lg[4]), "+v"(lg[5]), "+v"(lg[6]), "+v"(lg[7]));
#pragma unroll
      for (int i = 0; i < 5; ++i) { o[i][0] = fmul(lg[0], kv[0][i][0]); o[i][1] = fmul(lg[0], kv[0][i][1]); o[i][2] = fmul(lg[0], kv[0][i][2]); o[i][3] = fmul(lg[0], kv[0][i][3]); }
#pragma unroll
      for (int w = 1; w < 8; ++w)
#pragma unroll
        for (int i = 0; i < 5; ++i) { fmac(o[i][0], lg[w], kv[w][i][0]); fmac(o[i][1], lg[w], kv[w][i][1]); fmac(o[i][2], lg[w], kv[w][i][2]); fmac(o[i][3], lg[w], kv[w][i][3]); }
      const unsigned sg = L_STG_A + g4 * 2304 + i16 * 16;
#pragma unroll
      for (int j = 0; j < 10; ++j) {
        const int pr = 9 * a + j - (a & 1);
        const bool junk = (a & 1) ? j == 0 : j == 9;
        lds_st<unsigned>(sg + (junk ? 9216 : (pr >> 2) * 256 + (pr & 3) * 4), pk2(o[j >> 1][2 * (j & 1)], o[j >> 1][2 * (j & 1) + 1]));
      }
    }
    lsync<1>(cnt, one, 48);
    TS(11);
    {
      const __amdgpu_buffer_rsrc_t ro = mk_rs(ws + WS_OA + (size_t)rb * 73728 + hg * 2304, 73728);
      publish_piece(ro, L_STG_A, a, lane); publish_piece(ro, L_STG_A, a + 4, lane); if (a == 0) publish_piece(ro, L_STG_A, 8, lane);
    }
    unsigned w0 = ws_voff(lane, a, RB), w1 = ws_voff(lane, a + 4, RB), w8 = ws_voff(lane, 8, RB); PIN(w0); PIN(w1); PIN(w8);
    WAITV(0);
    lsync<1>(cnt, one, 52);
    if (wid == 4 && lane == 0) __hip_atomic_store((GAS unsigned*)(ws + WS_OAFLAG) + g8, tag, __ATOMIC_RELAXED, __HIP_MEMORY_SCOPE_AGENT);
    load_wo(A.Awo, hg, koff, a, w0, w1, w8);
    wait_flags(ws + WS_OAFLAG + rb * 32, 8, tag, lane);
    TS(12);
    {
      const int mt = a & 1, kh = a >> 1;
      const __amdgpu_buffer_rsrc_t ro = mk_rs(ws + WS_OA + (size_t)rb * 73728 + mt * 36864 + kh * 9216, 36864);
      const unsigned ov = (r32 >> 4) * 18432 + hi * 256 + i16 * 16;
      u32x4 of[18];
#pragma unroll
      for (int ks = 0; ks < 18; ++ks) of[ks] = __builtin_amdgcn_raw_buffer_load_b128(ro, ov + (ks & 7) * 512, (ks >> 3) * 4096, 16);
      unsigned wa = L_WO + (kh * 288 + 8 * hi + tq) * 144 + (g4 & 1) * 32 + tp * 8, w2 = L_WO + (kh * 288 + 8 * hi + tq) * 144 + 128 + (tp & 1) * 8; PIN(wa); PIN(w2);
      lsync<1>(cnt, one, 56);
      PRIO(0);
      bf16x8 wf[2][3];
#define OREAD(ks, bf) do { wf[bf][0] = trd2(wa + (ks) * 2304, 576); wf[bf][1] = trd2(wa + (ks) * 2304 + 64, 576); wf[bf][2] = trd2(w2 + (ks) * 2304, 576); SB(); } while (0)
      OREAD(0, 0);
#pragma unroll
      for (int ks = 0; ks < 18; ++ks) {
        if (ks + 1 < 18) OREAD(ks + 1, (ks + 1) & 1);
#pragma unroll
        for (int n = 0; n < 3; ++n) acc[n] = MFMA16(wf[ks & 1][n], BC8(of[ks]), ks ? acc[n] : zero);
        SB();
      }
      PRIO(1);
      const unsigned rd = L_RED_A + mt * 9216 + lane * 16;
      if (kh) {
#pragma unroll
        for (int i = 0; i < 9; ++i) lds_st<f32x4>(rd + i * 1024, f32x4{acc[i >> 2][4 * (i & 3)], acc[i >> 2][4 * (i & 3) + 1], acc[i >> 2][4 * (i & 3) + 2], acc[i >> 2][4 * (i & 3) + 3]});
      }
      lsync<1>(cnt, one, 60);
      if (!kh) {
        const __amdgpu_buffer_rsrc_t rt = mk_rs(A.out + (size_t)(rb * 64 + mt * 32) * (2 * D) + hg * 72, 32 * 2 * RB);
#pragma unroll
        for (int i = 0; i < 9; ++i) { const f32x4 v = lds_ld<f32x4>(rd + i * 1024); const int n = i >> 2, g = i & 3;
          float t0 = acc[n][4 * g] + v[0], t1 = acc[n][4 * g + 1] + v[1], t2 = acc[n][4 * g + 2] + v[2], t3 = acc[n][4 * g + 3] + v[3]; PIN(t0); PIN(t2);
          __builtin_amdgcn_raw_buffer_store_b64(u32x2{pk2(t0, t1), pk2(t2, t3)}, rt, r32 * 2 * RB + hi * 8 + i * 16, 0, 0); }
      }
    }
    PRIO(0);
    load_wo(A.Bwo, hg, koff, a, w0, w1, w8);
    WAITV(0);
    TS(13);
    lsync<4>(cntj, one, 8);
  }
  BAR();
  TS(14);
  if (b == 0 && tid == 0) __hip_atomic_store((GAS unsigned*)(ws + WS_EPOCH), tag, __ATOMIC_RELAXED, __HIP_MEMORY_SCOPE_AGENT);
  {
    float of[12];
#pragma unroll
    for (int c = 0; c < 3; ++c) { const f32x4 u = lds_ld<f32x4>(L_CMB + wid * 6144 + c * 1024 + lane * 16), v = lds_ld<f32x4>(L_CMB + wid * 6144 + 3072 + c * 1024 + lane * 16);
      of[4 * c] = u[0] + v[0]; of[4 * c + 1] = u[1] + v[1]; of[4 * c + 2] = u[2] + v[2]; of[4 * c + 3] = u[3] + v[3]; }
    float l = lowhalf(of[10]);
    float asum = 0.f;
#pragma unroll
    for (int i = 0; i < 10; ++i) asum += fabsf(of[i]);
    const bool ok = (l > 3.0e-6f) && (l < 1.0e37f) && (asum < 1.0e37f);
    if (__builtin_expect(__builtin_amdgcn_readfirstlane(__any(!ok)), 0)) {
      const __amdgpu_buffer_rsrc_t ri = mk_rs(ws + WS_IMG, 64 * TILE_B);
#define LDK1(t) __builtin_amdgcn_raw_buffer_load_b128(ri, vk1, (t) * TILE_B, 16)
#define LDK2(t) __builtin_amdgcn_raw_buffer_load_b64(ri, vk2, (t) * TILE_B, 16)
#define LDV0(t) __builtin_amdgcn_raw_buffer_load_b128(ri, vv, (t) * TILE_B, 16)
#define LDV1(t) __builtin_amdgcn_raw_buffer_load_b128(ri, vv + 2 * BLK_B, (t) * TILE_B, 16)
      float m = -3.0e38f;
#pragma unroll 1
      for (int t = 0; t < 64; ++t) { f32x16 s = MFMA16(BC8(LDK1(t)), BC8(q1o), zero); s = MFMA8(BC4(LDK2(t)), BC4(q2o), s);
#pragma unroll
        for (int i = 0; i < 16; i += 2) m = fmaxf(fmaxf(m, s[i]), s[i + 1]); }
      m = swapmax(m);
      f32x16 os = zero;
#pragma unroll 1
      for (int t = 0; t < 64; ++t) {
        f32x16 s = MFMA16(BC8(LDK1(t)), BC8(q1o), zero); s = MFMA8(BC4(LDK2(t)), BC4(q2o), s);
        u32x4 p0, p1;
#pragma unroll
        for (int i = 0; i < 4; ++i) { p0[i] = pk2(EX(s[2 * i] - m), EX(s[2 * i + 1] - m)); p1[i] = pk2(EX(s[8 + 2 * i] - m), EX(s[9 + 2 * i] - m)); }
        os = MFMA16(BC8(LDV0(t)), BC8(p0), os); os = MFMA16(BC8(LDV1(t)), BC8(p1), os);
      }
#pragma unroll
      for (int i = 0; i < 12; ++i) of[i] = os[i];
      l = lowhalf(of[10]);
    }
    const float rl = 1.f / l;
    const unsigned sg = L_STG_B + (qh * 2 + (r32 >> 4)) * 2304 + i16 * 16;
#pragma unroll
    for (int i = 0; i < 5; ++i) {
      const int c = hl * 18 + (i >> 1) * 8 + (i & 1) * 2 + 4 * hi;
      if (i < 4 || !hi) lds_st<unsigned>(sg + (c >> 3) * 256 + (c & 7) * 2, pk2(of[2 * i] * rl, of[2 * i + 1] * rl));
    }
  }
  BAR();
  {
    const __amdgpu_buffer_rsrc_t ro = mk_rs(ws + WS_OB + (size_t)rb * 73728 + hg * 2304, 73728);
    publish_piece(ro, L_STG_B, wid, lane); if (wid == 7) publish_piece(ro, L_STG_B, 8, lane);
  }
  const int mq = wid & 3, kh = wid >> 2;
  const unsigned wa = L_WO + (kh * 288 + 8 * g4 + tq) * 144 + tp * 8, w4 = tp < 2 ? wa + 128 : wa + 112;
  bf16x8 wf[9][5];
#pragma unroll
  for (int ks = 0; ks < 4; ++ks)
#pragma unroll
    for (int n = 0; n < 5; ++n) wf[ks][n] = trd2((n < 4 ? wa + n * 32 : w4) + ks * 4608, 576);
  SB();
  WAITV(0);
  asm volatile("s_barrier" ::: "memory");
  if (tid == 0) __hip_atomic_store((GAS unsigned*)(ws + WS_OBFLAG) + g8, tag, __ATOMIC_RELAXED, __HIP_MEMORY_SCOPE_AGENT);
  TS(15);
  {
#pragma unroll
    for (int ks = 4; ks < 9; ++ks)
#pragma unroll
      for (int n = 0; n < 5; ++n) wf[ks][n] = trd2((n < 4 ? wa + n * 32 : w4) + ks * 4608, 576);
    SB();
    wait_flags(ws + WS_OBFLAG + rb * 32, 8, tag, lane);
    TS(16);
    const __amdgpu_buffer_rsrc_t ro = mk_rs(ws + WS_OB + (size_t)rb * 73728 + mq * 18432 + kh * 9216, 9216);
    u32x4 of[9];
#pragma unroll
    for (int ks = 0; ks < 9; ++ks) of[ks] = __builtin_amdgcn_raw_buffer_load_b128(ro, lane * 16 + (ks & 3) * 1024, (ks >> 2) * 4096, 16);
    f32x4 oc[5];
#pragma unroll
    for (int n = 0; n < 5; ++n) oc[n] = f32x4{0.f, 0.f, 0.f, 0.f};
#pragma unroll
    for (int ks = 0; ks < 9; ++ks)
#pragma unroll
      for (int n = 0; n < 5; ++n) oc[n] = MF16(wf[ks][n], BC8(of[ks]), oc[n]);
    TS(17);
    if (kh) {
#pragma unroll
      for (int n = 0; n < 5; ++n) lds_st<f32x4>(L_RED + (mq * 5 + n) * 1024 + lane * 16, oc[n]);
    }
    BAR();
    if (!kh) {
      const __amdgpu_buffer_rsrc_t rt = mk_rs(A.out + (size_t)(rb * 64 + mq * 16) * (2 * D) + D + hg * 72, 16 * 2 * RB - RB);
      const unsigned tv = i16 * 2 * RB + g4 * 8;
#pragma unroll
      for (int n = 0; n < 5; ++n) { const f32x4 v = oc[n] + lds_ld<f32x4>(L_RED + (mq * 5 + n) * 1024 + lane * 16);
        __builtin_amdgcn_raw_buffer_store_b64(u32x2{pk2(v[0], v[1]), pk2(v[2], v[3])}, rt, (n < 4 || g4 < 2) ? tv + n * 32 : OOB, 0, 0); }
    }
  }
  TS(18);
}

extern "C" void kernel_launch(void* const* d_in, const int* in_sizes, int n_in, void* d_out, int out_size, void* d_ws, size_t ws_size, hipStream_t stream) {
  Args a;
  a.Ax = (const u16*)d_in[0]; a.Awqkv = (const u16*)d_in[1]; a.Awo = (const u16*)d_in[2];
  a.Bx = (const u16*)d_in[3]; a.Bwq = (const u16*)d_in[4]; a.Bwk = (const u16*)d_in[5]; a.Bwv = (const u16*)d_in[6]; a.Bwo = (const u16*)d_in[7];
  a.ws = (char*)d_ws; a.out = (u16*)d_out;
  hipFuncSetAttribute((const void*)k_fused, hipFuncAttributeMaxDynamicSharedMemorySize, 160 * 1024);
  k_fused<<<256, 512, 160 * 1024, stream>>>(a);
}
```

```cpp
#include <hip/hip_runtime.h>
#include <hip/hip_bf16.h>
#include <stdint.h>

typedef __attribute__((ext_vector_type(8))) __bf16 bf16x8;
typedef __attribute__((ext_vector_type(4))) short s16x4;
typedef __attribute__((ext_vector_type(8))) short s16x8;
typedef __attribute__((ext_vector_type(2))) __bf16 bf16x2;
typedef __attribute__((ext_vector_type(2))) float f32x2;
typedef __attribute__((ext_vector_type(16))) float f32x16;
typedef __attribute__((ext_vector_type(4))) float f32x4;
typedef __attribute__((ext_vector_type(4))) unsigned u32x4;
typedef __attribute__((ext_vector_type(2))) unsigned u32x2;
typedef unsigned short u16;
#define LAS __attribute__((address_space(3)))
#define GAS __attribute__((address_space(1)))
#define DI __device__ __forceinline__
#define SB() __builtin_amdgcn_sched_barrier(0)
#define BC8(x) __builtin_bit_cast(bf16x8, x)
#define BC4(x) __builtin_bit_cast(s16x4, x)
#define MFMA16(a, b, c) __builtin_amdgcn_mfma_f32_32x32x16_bf16(a, b, c, 0, 0, 0)
#define MFMA8(a, b, c) __builtin_amdgcn_mfma_f32_32x32x8bf16_1k(a, b, c, 0, 0, 0)
#define VMFMA(acc, a, b) asm volatile("v_mfma_f32_32x32x16_bf16 %0, %1, %2, %0" : "+v"(acc) : "v"(a), "v"(b))
#define MF16(a, b, c) __builtin_amdgcn_mfma_f32_16x16x32_bf16(a, b, c, 0, 0, 0)
#define EX(x) __builtin_amdgcn_exp2f(x)
#define WAITV_(n) asm volatile("s_waitcnt vmcnt(" #n ")" ::: "memory")
#define WAITV(n) WAITV_(n)
#define BAR() asm volatile("s_waitcnt lgkmcnt(0)\n\ts_barrier" ::: "memory")
#define PRIO(n) do { SB(); __builtin_amdgcn_s_setprio(n); SB(); } while (0)
#define PIN(x) asm volatile("" : "+v"(x))
#define PACE 20

constexpr int S = 2048, D = 576, DH = 18, RB = D * 2;
constexpr float QSCALE = 0.23570226039551584f * 1.4426950408889634f;
constexpr int BLK_B = 640, TILE_B = 4 * BLK_B, K2_OFF = 256, V_OFF = 320;
constexpr size_t WS_EPOCH = 0, WS_KVFLAG = 4096, WS_OAFLAG = 12288, WS_OBFLAG = 20480, WS_IMG = 32768, WS_OA = 262144, WS_OB = WS_OA + (size_t)S * RB;
constexpr int SPIN_MAX = 1 << 17;
constexpr unsigned CODE_B = 21504;
constexpr unsigned OOB = 0x40000000u;
constexpr int L_R0 = 0, L_D = 21504, L_XB = 27648, L_WQ4 = 101376, L_WQ0 = 119808, L_MISC = 161792;
#define WQPOS(p) ((p) < 4 ? L_WQ0 + (p) * 9216 : ((p) < 6 ? L_WQ4 + ((p) - 4) * 9216 : L_R0 + ((p) - 6) * 9216))
constexpr int L_KV = 119808 - 2 * TILE_B, KVS_B = 2 * TILE_B, L_KV00 = 156672, L_QX = 146432, QX_B = 1280;
#define KVSLOT(h, s) ((((h) ^ h0) | (s)) == 0 ? L_KV00 : L_KV + (((h) ^ h0) * 3 + (s)) * KVS_B)
constexpr int SLOT_B = 37888, SLOT_X = 27648, F32_S = 912;
constexpr int L_WO = 0, L_STG_A = 73728, L_WA5 = 82944;
constexpr int L_CMB = 119808, L_STG_B = 140288, L_RED = 83968;
enum { C_KVIN, C_R0, C_KVP, C_QS0, C_QS1, C_QS2, C_QS3, C_QS4, C_QD, C_QD2, C_QX, C_KVF0, C_KVF1, C_KVF2, C_KVF3, C_KVC0, C_KVC1, C_KVC2, C_KVC3, C_AF0, C_AF1, C_AD0, C_AD1, C_AD2, C_AD3, C_KVS, C_AX, C_WO, C_OAF, C_JOIN, C_OBF, C_TAG };
#define CA(i) (L_MISC + 4 * (i))
typedef char loop_immediates_match[(4 * C_KVF0 == 44 && 4 * C_KVC0 == 60 && TILE_B == 2560 && 2 * BLK_B == 1280) ? 1 : -1];

DI unsigned pk2(float a, float b) { f32x2 v = {a, b}; bf16x2 r = __builtin_convertvector(v, bf16x2); return __builtin_bit_cast(unsigned, r); }
DI float bflo(unsigned u) { return __uint_as_float(u << 16); }
DI s16x4 trd(unsigned a) { return __builtin_amdgcn_ds_read_tr16_b64_v4i16((LAS s16x4*)(uintptr_t)a); }
DI bf16x8 cat(s16x4 a, s16x4 b) { s16x8 v = __builtin_shufflevector(a, b, 0, 1, 2, 3, 4, 5, 6, 7); return BC8(v); }
DI bf16x8 trd2(unsigned a, int second) { return cat(trd(a), trd(a + second)); }
template <class T> DI T lds_ld(unsigned a) { return *(const LAS T*)(uintptr_t)a; }
template <class T> DI void lds_st(unsigned a, T v) { *(LAS T*)(uintptr_t)a = v; }
DI u32x4 mk_rsrc(const void* p, unsigned bytes) { unsigned long long a = (unsigned long long)p; return u32x4{(unsigned)a, (unsigned)(a >> 32) & 0xffffu, bytes, 0x00020000u}; }
DI __amdgpu_buffer_rsrc_t mk_rs(const void* p, unsigned bytes) { return __builtin_amdgcn_make_buffer_rsrc((void*)p, 0, bytes, 0x00020000); }
DI void dma16(u32x4 rs, unsigned lds, unsigned voff, unsigned soff) {
  asm volatile("s_mov_b32 m0, %0\n\ts_nop 0\n\tbuffer_load_dwordx4 %1, %2, %3 offen lds" :: "s"(lds), "v"(voff), "s"(rs), "s"(soff) : "memory");
}
DI float swapmax(float m) { auto rr = __builtin_amdgcn_permlane32_swap(__float_as_uint(m), __float_as_uint(m), false, false); return fmaxf(__uint_as_float(rr[0]), __uint_as_float(rr[1])); }
DI float lowhalf(float m) { auto rr = __builtin_amdgcn_permlane32_swap(__float_as_uint(m), __float_as_uint(m), false, false); return __uint_as_float(rr[0]); }
DI unsigned xt_voff(int lane, int octpar) { return (unsigned)((lane >> 3) * RB + (((lane & 7) ^ ((4 * octpar + (lane >> 4)) & 7)) << 4)); }
DI unsigned ws_voff(int lane, int i, int ld) { const int idx = 64 * i + lane, row = (idx * 7282) >> 16; return (unsigned)(row * (ld - 144) + idx * 16); }
template <int SLP> DI void wait_flags(const char* flags, unsigned tag, int lane) {
  const __amdgpu_buffer_rsrc_t rf = mk_rs(flags, 32);
#pragma unroll 1
  for (int spin = 0; spin < SPIN_MAX; ++spin) {
    const unsigned f = __builtin_amdgcn_raw_buffer_load_b32(rf, (lane & 7) * 4, 0, 16);
    if (__all(f == tag)) break;
    __builtin_amdgcn_s_sleep(SLP);
    asm volatile("" ::: "memory");
  }
  asm volatile("" ::: "memory");
}
template <int SLP> DI void wait_flags16(const char* flags, unsigned tag, int lane) {
  const __amdgpu_buffer_rsrc_t rf = mk_rs(flags, 4096);
#pragma unroll 1
  for (int spin = 0; spin < SPIN_MAX; ++spin) {
    const u32x2 f = __builtin_amdgcn_raw_buffer_load_b64(rf, (lane >> 2) * 256 + (lane & 3) * 8, 0, 16);
    if (__all(f[0] == tag && f[1] == tag)) break;
    __builtin_amdgcn_s_sleep(SLP);
    asm volatile("" ::: "memory");
  }
  asm volatile("" ::: "memory");
}
#define FLAG(base) ((GAS unsigned*)(ws + (base) + rb * 256) + hg)
#define CSIG(i) asm volatile("s_waitcnt lgkmcnt(0)\n\ts_mov_b64 exec, 1\n\tds_add_u32 %0, %1 offset:%2\n\ts_mov_b64 exec, -1" :: "v"(mb), "v"(one), "i"(4 * (i)) : "memory")
template <int SLP, int OFF> DI void cwait_(unsigned mb, unsigned target) {
  unsigned tv, ts, tc;
  asm volatile("s_mov_b32 %2, 0x100000\n"
               "1:\n\tds_read_b32 %0, %3 offset:%6\n\ts_waitcnt lgkmcnt(0)\n\tv_readfirstlane_b32 %1, %0\n\ts_nop 0\n\ts_cmp_ge_u32 %1, %4\n\ts_cbranch_scc1 2f\n\t"
               "s_sleep %5\n\ts_sub_u32 %2, %2, 1\n\ts_cbranch_scc0 1b\n2:"
               : "=&v"(tv), "=&s"(ts), "=&s"(tc) : "v"(mb), "s"(target), "i"(SLP), "i"(OFF) : "memory", "scc");
}
template <int SLP> DI void cwaitn(unsigned va, unsigned target) {
  unsigned tv, tc;
  asm volatile("s_mov_b32 %1, 0x100000\n"
               "1:\n\tds_read_b32 %0, %2\n\ts_waitcnt lgkmcnt(0)\n\tv_cmp_gt_u32 vcc, %3, %0\n\ts_nop 4\n\ts_cbranch_vccz 2f\n\t"
               "s_sleep %4\n\ts_sub_u32 %1, %1, 1\n\ts_cbranch_scc0 1b\n2:"
               : "=&v"(tv), "=&s"(tc) : "v"(va), "s"(target), "i"(SLP) : "memory", "scc", "vcc");
}
#define CSIGV(va, off) asm volatile("s_waitcnt lgkmcnt(0)\n\ts_mov_b64 exec, 1\n\tds_add_u32 %0, %1 offset:%2\n\ts_mov_b64 exec, -1" :: "v"(va), "v"(one), "i"(off) : "memory")
#define CWAIT(i, target, slp) cwait_<slp, 4 * (i)>(mb, target)
#define CSYNC(i, target, slp) do { CSIG(i); CWAIT(i, target, slp); } while (0)
DI void dma16s(u32x4 rs, unsigned lds, unsigned voff, unsigned soff) {
  asm volatile("s_mov_b32 m0, %0\n\ts_nop 0\n\tbuffer_load_dwordx4 %1, %2, %3 offen sc1 lds" :: "s"(lds), "v"(voff), "s"(rs), "s"(soff) : "memory");
}
DI void touch(const void* p, unsigned bytes, unsigned off, unsigned n, int lane) {
  const u32x4 rs = mk_rsrc((const char*)p + off, n < bytes - off ? n : bytes - off);
#pragma unroll 1
  for (unsigned o = 0; o < n; o += 8192) asm volatile("s_mov_b32 m0, %0\n\ts_nop 0\n\tbuffer_load_dword %1, %2, %3 offen lds" :: "s"(L_MISC + 1792), "v"(lane * 128), "s"(rs), "s"(o) : "memory");
}
DI float fmul(float a, float b) { float r; asm("v_mul_f32 %0, %1, %2" : "=v"(r) : "v"(a), "v"(b)); return r; }

DI void piece_addr(int i, int lane, int pitch, unsigned& la, unsigned& go) {
  const int idx = 64 * i + lane, ch = idx >> 4, m16 = (ch * 57) >> 9;
  la = (unsigned)((m16 * 16 + (lane & 15)) * pitch + (ch - m16 * 9) * 16); go = (unsigned)(m16 * (18432 - 2304) + idx * 16);
}
#define LANES() int lane = lane0; PIN(lane); const int r32 = lane & 31, hi = lane >> 5, g4 = lane >> 4, i16 = lane & 15, tq = i16 >> 2, tp = i16 & 3; (void)r32; (void)hi; (void)g4; (void)i16; (void)tq; (void)tp
#define KVOFFS() const int kblk = 2 * (r32 >> 4) + ((r32 >> 2) & 1), kj = 4 * ((r32 >> 3) & 1) + (r32 & 3); \
  const unsigned vk1 = kblk * BLK_B + (kj * 2 + hi) * 16, vk2 = kblk * BLK_B + K2_OFF + kj * 8; \
  const unsigned vv = r32 < 19 ? (unsigned)(hi * BLK_B + V_OFF + r32 * 16) : OOB
struct Args { const u16* Ax; const u16* Awqkv; const u16* Awo; const u16* Bx; const u16* Bwq; const u16* Bwk; const u16* Bwv; const u16* Bwo; char* ws; u16* out; };

__global__ __launch_bounds__(768, 3) void k_fused(Args A) {
  extern __shared__ __attribute__((aligned(16))) char smem[];
  asm volatile("" :: "v"((LAS char*)smem));
  char* const ws = A.ws;
  const int wid = __builtin_amdgcn_readfirstlane(threadIdx.x >> 6);
  int lane0 = threadIdx.x & 63; PIN(lane0);
  const int b = blockIdx.x, xcd = b & 7, jj = b >> 3, rb = xcd * 4 + (jj >> 3), hg = jj & 7, g8 = rb * 8 + hg;
  const int koff = (2 * (rb & 3) + (hg >> 1)) % 9, oq = hg & 6, h0 = rb >> 4;
#define KNEXT(k) ((k) == 8 ? 0 : (k) + 1)
  unsigned one = 1u, mb = L_MISC; PIN(one); PIN(mb);
  if (wid == 2) {
    unsigned long long pc; asm volatile("s_getpc_b64 %0" : "=s"(pc));
    touch((const void*)(pc & ~127ull), CODE_B, 0, CODE_B, lane0);
  }
  if (wid == 0 && lane0 < C_TAG) lds_st<unsigned>(L_MISC + lane0 * 4, 0u);
  asm volatile("" :: "s"(A.Bx), "s"(A.Bwk), "s"(A.Bwv), "s"(A.Bwq));
  BAR();

  if (__builtin_expect(wid >= 8, 1)) {
    const int l = wid - 8, lane = lane0;
    PRIO(3);
    const u32x4 rs_x = mk_rsrc(A.Bx + (size_t)rb * 64 * D, 64 * RB), rs_wq = mk_rsrc(A.Bwq + hg * 72, (D * D - hg * 72) * 2);
    const unsigned xe = xt_voff(lane, 0), xo = xe ^ 64;
    {
      if (l == 0) { const u32x4 rs_e = mk_rsrc(ws + WS_EPOCH, 4); const unsigned z = 0;
        asm volatile("s_mov_b32 m0, %0\n\ts_mov_b64 exec, 1\n\tbuffer_load_dword %1, %2, 0 offen lds\n\ts_mov_b64 exec, -1" :: "s"(CA(C_TAG)), "v"(z), "s"(rs_e) : "memory"); }
      const u32x4 rs_w = mk_rsrc((hg & 1) ? A.Bwv : A.Bwk, D * DH * 2);
      int kx = l + koff;
#pragma unroll 1
      for (int p = l; p < 9; p += 4) { kx = kx >= 9 ? kx - 9 : kx; const unsigned so = oq * 8 * RB + kx * 128; dma16(rs_x, L_XB + (p * 8 + oq) * 1024, xe, so); dma16(rs_x, L_XB + (p * 8 + oq + 1) * 1024, xo, so + 8 * RB); kx += 4; }
      int ii = l + (jj >> 1);
#pragma unroll 1
      for (int i = l; i < 21; i += 4) { dma16(rs_w, L_R0 + ii * 1024, lane * 16, ii * 1024); ii = ii >= 17 ? ii - 17 : ii + 4; }
    }
    if (l < 3) {
      const int o0 = (oq + 2 + 2 * l) & 7;
      unsigned ml = mb + 4 * l; PIN(ml);
      const unsigned w0 = ws_voff(lane, 3 * l, RB), w1 = ws_voff(lane, 3 * l + 1, RB), w2 = ws_voff(lane, 3 * l + 2, RB);
      int kc = koff;
#pragma unroll 1
      for (int p = 0; p < 9; ++p) {
        dma16(rs_x, L_XB + (p * 8 + o0) * 1024, xe, o0 * 8 * RB + kc * 128); dma16(rs_x, L_XB + (p * 8 + o0 + 1) * 1024, xo, (o0 + 1) * 8 * RB + kc * 128);
        if (p == 6) CWAIT(C_KVP, 4, 0);
        const unsigned wd = WQPOS(p) + 3 * l * 1024;
        dma16(rs_wq, wd, w0, kc * 64 * RB); dma16(rs_wq, wd + 1024, w1, kc * 64 * RB);
        dma16(rs_wq, wd + 2048, w2, kc * 64 * RB);
        WAITV(5);
        if (p == 0) { CSIG(C_KVIN); } else CSIGV(ml, 4 * C_QS0);
        if (p == 7) CWAIT(C_KVS, 1, 0);
        kc = KNEXT(kc);
      }
      CWAIT(C_QD, 4, 0);
      WAITV(0); CSIGV(ml, 4 * C_QS0);
    } else {
      const int bk = lane >= 20, c20 = lane - 20 * bk;
      const bool isv = hg & 1, k2c = !isv && c20 >= 16, ones = isv && c20 >= 18;
      const int a0 = isv ? (c20 > 17 ? 17 : c20) : (c20 < 16 ? (c20 >> 1) * 20 + (c20 & 1) * 10 : (c20 & 3) * 40 + 8), st = isv ? 80 : 4;
      unsigned da[8];
#pragma unroll
      for (int e = 0; e < 8; ++e) { da[e] = L_D + bk * 1280 + a0 * 4 + e * st + ((k2c && e >= 4) ? 64 : 0); PIN(da[e]); }
      unsigned sv = lane < 40 ? bk * BLK_B + (isv ? V_OFF : 0) + c20 * 16 : OOB; PIN(sv);
      WAITV(0); CSIG(C_KVIN);
      CWAIT(C_KVP, 4, 0);
      float v[8];
#pragma unroll
      for (int e = 0; e < 8; ++e) v[e] = (lds_ld<float>(da[e]) + lds_ld<float>(da[e] + 648)) + (lds_ld<float>(da[e] + 2560) + lds_ld<float>(da[e] + 3208));
      const unsigned tg = lds_ld<unsigned>(CA(C_TAG)) + 1u;
      u32x4 c = {pk2(v[0], v[1]), pk2(v[2], v[3]), pk2(v[4], v[5]), pk2(v[6], v[7])};
      if (k2c || ones) { c[1] = 0x3f803f80u; c[3] = 0x3f803f80u; }
      if (ones) { c[0] = 0x3f803f80u; c[2] = 0x3f803f80u; }
      __builtin_amdgcn_raw_buffer_store_b128(c, mk_rs(ws + WS_IMG + (size_t)(rb * 8 + oq) * BLK_B, 2 * BLK_B), sv, 0, 16);
      CSIG(C_KVS);
      WAITV(0);
      if (lane == 0) __hip_atomic_store(FLAG(WS_KVFLAG), tg, __ATOMIC_RELAXED, __HIP_MEMORY_SCOPE_AGENT);
      {
        const u32x4 rs_i = mk_rsrc(ws + WS_IMG, 64 * TILE_B);
        wait_flags<2>(ws + WS_KVFLAG + rb * 256, tg, lane);
#pragma unroll
        for (int u = 0; u < 5; ++u) dma16s(rs_i, L_KV00 + u * 1024, lane * 16, rb * KVS_B + u * 1024);
        WAITV(0);
        asm volatile("s_mov_b64 exec, 3\n\tds_add_u32 %0, %1\n\ts_mov_b64 exec, -1" :: "v"(CA(C_KVF0) + h0 * 8 + (lane & 1) * 4), "v"(one) : "memory");
        CSIG(C_R0);
        wait_flags<2>(ws + WS_KVFLAG + (rb ^ 16) * 256, tg, lane);
        CWAIT(C_QD, 4, 0);
#pragma unroll
        for (int u = 0; u < 5; ++u) dma16s(rs_i, KVSLOT(h0 ^ 1, 0) + u * 1024, lane * 16, (rb ^ 16) * KVS_B + u * 1024);
        CSIG(C_KVS);
        WAITV(0);
        asm volatile("s_mov_b64 exec, 3\n\tds_add_u32 %0, %1\n\ts_mov_b64 exec, -1" :: "v"(CA(C_KVF0) + (h0 ^ 1) * 8 + (lane & 1) * 4), "v"(one) : "memory");
      }
    }
    const unsigned tag = lds_ld<unsigned>(CA(C_TAG)) + 1u;
    if (l < 2) {
      const u32x4 rs_i = mk_rsrc(ws + WS_IMG, 64 * TILE_B);
      unsigned vc = CA(C_KVC0 + 2 * l) + (lane & 1) * 4; PIN(vc);
#define KVSIG() asm volatile("s_mov_b64 exec, 3\n\tds_add_u32 %0, %1\n\ts_mov_b64 exec, -1" :: "v"(vc - 16), "v"(one) : "memory")
      CWAIT(C_KVS, 2, 1);
      wait_flags16<4>(ws + WS_KVFLAG + l * 4096, tag, lane);
      int pr = (rb + 1) & 15, sl = 1;
#pragma unroll 1
      for (int i = 1; i < 16; ++i) {
        if (i >= 3) cwaitn<10>(vc, i - 2);
        const unsigned dst = KVSLOT(l, sl), so = (l * 16 + pr) * KVS_B;
#pragma unroll
        for (int u = 0; u < 5; ++u) dma16s(rs_i, dst + u * 1024, lane * 16, so + u * 1024);
        if (i == 2) { CSIG(C_KVS); cwaitn<1>(vc - 16, 1); WAITV(5); KVSIG(); }
        if (i >= 2) { WAITV(0); KVSIG(); }
        pr = (pr + 1) & 15; sl = sl == 2 ? 0 : sl + 1;
      }
      KVSIG();
    } else {
      const int m = l - 2;
      unsigned wv_[5];
#pragma unroll
      for (int u = 0; u < 5; ++u) wv_[u] = ws_voff(lane, 2 * u + m, 3 * RB);
      const u32x4 rs_xa = mk_rsrc(A.Ax + ((size_t)rb * 64 - 16) * D, 80 * RB), rs_w = mk_rsrc(A.Awqkv + hg * 72, (D * 3 * D - hg * 72) * 2);
      unsigned vd = CA(C_AD0) + (lane & 3) * 4, mm = mb + 4 * m; PIN(vd); PIN(mm);
      CWAIT(C_QD2, 4, 1);
      CWAIT(C_KVS, 4, 1);
      int kc = 2 * (rb & 3), sl = 0;
#pragma unroll 1
      for (int c = 0; c < 9; ++c) {
        if (c >= 3) cwaitn<3>(vd, c - 2);
        const unsigned sb = sl * SLOT_B;
#pragma unroll 1
        for (int oc = m ? 4 : 0; oc < (m ? 10 : 4); oc += 2) {
          const unsigned so = (rb == 0 && oc == 0) ? OOB : oc * 8 * RB + kc * 128;
          dma16(rs_xa, sb + SLOT_X + oc * 1024, xe, so); dma16(rs_xa, sb + SLOT_X + oc * 1024 + 1024, xo, so + 8 * RB); WAITV(PACE);
        }
#pragma unroll 1
        for (int sg = 0; sg < 3; ++sg) {
          const unsigned wd = sb + sg * 9216 + m * 1024, so = kc * 64 * 3 * RB + sg * RB;
#pragma unroll
          for (int u = 0; u < 4; ++u) { dma16(rs_w, wd + u * 2048, wv_[u], so); WAITV(PACE); }
          if (m == 0) dma16(rs_w, wd + 8192, wv_[4], so);
        }
        if (c == 0) { WAITV(0); CSIGV(mm, 4 * C_AF0); } else if (c >= 2) { WAITV(18); CSIGV(mm, 4 * C_AF0); }
        kc = KNEXT(kc); sl = sl == 2 ? 0 : sl + 1;
      }
      WAITV(0); CSIGV(mm, 4 * C_AF0);
#pragma unroll
      for (int u = 0; u < 5; ++u) wv_[u] = ws_voff(lane, 2 * u + m, RB);
      const u32x4 rs_a = mk_rsrc(A.Awo + hg * 72, (D * D - hg * 72) * 2), rs_b = mk_rsrc(A.Bwo + hg * 72, (D * D - hg * 72) * 2);
#define WCHUNKS(rs, c0, c1, base) do { _Pragma("unroll 1") for (int i_ = 0; i_ < (c1) - (c0); ++i_) { int c_ = i_ + (rb & 3); c_ = (c0) + (c_ >= (c1) - (c0) ? c_ - ((c1) - (c0)) : c_); const unsigned wd = (base) + c_ * 9216 + m * 1024, so = c_ * 64 * RB; \
          _Pragma("unroll") for (int u = 0; u < 4; ++u) { dma16(rs, wd + u * 2048, wv_[u], so); WAITV(PACE); } \
          if (m == 0) dma16(rs, wd + 8192, wv_[4], so); } } while (0)
      touch(m ? A.Bwo : A.Awo, D * D * 2, jj * 20736, 20736, lane);
      cwaitn<6>(vd, 9);
      WCHUNKS(rs_a, 5, 9, L_WA5 - 5 * 9216);
      CWAIT(C_AX, 8, 4);
      WCHUNKS(rs_a, 0, 5, L_WO);
      CWAIT(C_OAF, 4, 2);
      WCHUNKS(rs_b, 5, 9, L_WO);
      WAITV(0);
      if (m == 0) wait_flags<4>(ws + WS_OAFLAG + rb * 256, tag, lane);
      CSIG(C_WO);
      CWAIT(C_AX, 12, 4);
      WCHUNKS(rs_b, 0, 5, L_WO);
      WAITV(0);
      CSIG(C_WO);
    }
    return;
  }

  f32x16 zero;
#pragma unroll
  for (int i = 0; i < 16; ++i) zero[i] = 0.f;
  unsigned tag = 0;
  const int qh = wid & 1, kh2 = (wid >> 1) & 1;
  if (wid < 4) {
    if (wid == 3) {
      asm volatile("s_mov_b32 s40, 17\n\ts_mov_b32 s45, 0\n\ts_mov_b64 exec, 0\n\ts_branch .Latt_entry\n.Latt_dry_ret:\n\ts_mov_b64 exec, -1\n\ts_nop 11"
                   ::: "memory", "scc", "s40", "s41", "s42", "s43", "s44", "s45", "v0", "v1", "v2", "v3", "v4", "v5", "v6", "v7", "v8", "v9", "v10", "v11", "v12", "v13", "v14", "v15", "v16", "v17", "v18", "v19", "v20", "v21", "v22", "v23", "v24", "v25", "v26", "v27", "v28", "v29", "v30", "v31", "v32", "v33", "v34", "v35", "v36", "v37", "v38", "v39", "v40", "v41", "v42", "v43", "v44", "v45", "v46", "v47", "v48", "v49", "v50", "v51", "v52", "v53", "v54", "v55", "v56", "v57", "v58", "v59", "v60", "v61", "v62", "v63", "v64", "v65", "v66", "v67", "v68", "v69", "v70", "v71", "v72", "v73", "v74", "v75", "v76", "v77", "v78", "v79", "v80", "v81", "v82", "v83", "v84", "v85", "v86", "v87", "v88", "v89", "v90", "v91", "v92", "v93", "v94", "v95");
    }
    LANES(); KVOFFS();
    unsigned mh = lane ? OOB : (unsigned)(L_MISC + 4 * wid);
    unsigned ak1 = KVSLOT(kh2, 0) + vk1, ak2 = KVSLOT(kh2, 0) + vk2, av = KVSLOT(kh2, 0) + vv, mk1 = L_KV00 + vk1, mk2 = L_KV00 + vk2;
    unsigned q1d = L_QX + qh * QX_B + lane * 16, q2d = hi ? OOB : (unsigned)(L_QX + qh * QX_B + 1024 + r32 * 8);
    unsigned q1s = L_QX + 2 * wid * QX_B + lane * 16, q2s = hi ? OOB : (unsigned)(L_QX + 2 * wid * QX_B + 1024 + r32 * 8);
    PIN(mh); PIN(ak1); PIN(ak2); PIN(av); PIN(mk1); PIN(mk2); PIN(q1d); PIN(q2d); PIN(q1s); PIN(q2s);
    {
    const int par = wid & 1;
    f32x16 qa[2] = {zero, zero};
    const int gg = 2 * (g4 & 1) + (tp >> 1), hh = tp & 1;
    int col = ((wid * 18) & ~3) + (hh ? (par ? 12 : 8) : 0) + 4 * gg; col = col > 68 ? 68 : col;
    unsigned wr = (8 * hi + tq) * 144 + col * 2, wb = wr + L_WQ4;
    unsigned xo[4];
#pragma unroll
    for (int s = 0; s < 4; ++s) { xo[s] = L_XB + r32 * 128 + (((2 * s + hi) ^ ((r32 >> 1) & 7)) << 4); PIN(xo[s]); }
    PIN(wb); PIN(wr);
    bf16x8 wf[2][4], xf[2][4][2];
#define QREAD(p) do { _Pragma("unroll") for (int s = 0; s < 4; ++s) { wf[(p) & 1][s] = trd2(((p) < 6 ? wb + (WQPOS(p) - L_WQ4) : wr + WQPOS(p)) + s * 2304, 576); \
        xf[(p) & 1][s][0] = lds_ld<bf16x8>(xo[s] + ((p) & 7) * 8192); xf[(p) & 1][s][1] = lds_ld<bf16x8>(xo[s] + ((p) & 7) * 8192 + 4096); } \
        if ((p) == 7) { _Pragma("unroll") for (int s = 0; s < 4; ++s) { xo[s] += 65536; PIN(xo[s]); } } SB(); } while (0)
#define QMMA(p) do { _Pragma("unroll") for (int s = 0; s < 4; ++s) { VMFMA(qa[0], wf[(p) & 1][s], xf[(p) & 1][s][0]); VMFMA(qa[1], wf[(p) & 1][s], xf[(p) & 1][s][1]); } SB(); } while (0)
    PIN(qa[0]); PIN(qa[1]);
    unsigned vq = CA(C_QS0) + (lane > 2 ? 0 : lane) * 4; PIN(vq);
    CWAIT(C_KVIN, 4, 1);
    cwaitn<0>(vq, 1);
    QREAD(0);
    tag = __builtin_amdgcn_readfirstlane(lds_ld<unsigned>(CA(C_TAG))) + 1u;
    cwaitn<0>(vq, 2); QREAD(1); QMMA(0);
    cwaitn<0>(vq, 3); QREAD(2); QMMA(1);
    cwaitn<0>(vq, 4); QREAD(3); QMMA(2);
    CSIG(C_QD);
    cwaitn<0>(vq, 5); QREAD(4); QMMA(3);
    cwaitn<0>(vq, 6); QREAD(5); QMMA(4);
    cwaitn<0>(vq, 7); QREAD(6); QMMA(5);
    cwaitn<0>(vq, 8); QREAD(7); QMMA(6);
    cwaitn<0>(vq, 9); QREAD(8); QMMA(7);
    CSIG(C_QD2);
    QMMA(8);
    CWAIT(C_R0, 1, 0);
    const u32x4 kA = lds_ld<u32x4>(mk1), kB = lds_ld<u32x4>(mk1 + TILE_B); const u32x2 k2A = lds_ld<u32x2>(mk2), k2B = lds_ld<u32x2>(mk2 + TILE_B);
    asm volatile("s_nop 5" : "+v"(qa[0]), "+v"(qa[1]));
    const bool o1 = (par ^ hi) != 0;
    u32x4 q1o[2]; u32x2 q2o[2];
#pragma unroll
    for (int h = 0; h < 2; ++h) {
      unsigned pk[6];
#pragma unroll
      for (int i = 0; i < 6; ++i) { float t0 = qa[h][2 * i] * QSCALE, t1 = qa[h][2 * i + 1] * QSCALE; PIN(t0); pk[i] = pk2(t0, t1); }
      q1o[h] = u32x4{o1 ? pk[1] : pk[0], o1 ? pk[2] : pk[1], o1 ? pk[3] : pk[2], o1 ? pk[4] : pk[3]};
      q2o[h] = u32x2{hi ? 0u : (o1 ? pk[5] : pk[4]), 0u};
      lds_st<u32x4>(q1s + h * QX_B, q1o[h]);
    }
    f32x16 s[2][2];
#pragma unroll
    for (int h = 0; h < 2; ++h) { s[h][0] = MFMA16(BC8(kA), BC8(q1o[h]), zero); s[h][1] = MFMA16(BC8(kB), BC8(q1o[h]), zero); s[h][0] = MFMA8(BC4(k2A), BC4(q2o[h]), s[h][0]); s[h][1] = MFMA8(BC4(k2B), BC4(q2o[h]), s[h][1]); }
#pragma unroll
    for (int h = 0; h < 2; ++h) {
      float m = fmaxf(s[h][0][0], s[h][1][0]);
#pragma unroll
      for (int i = 1; i < 16; ++i) m = fmaxf(fmaxf(m, s[h][0][i]), s[h][1][i]);
      m = -(swapmax(m) + 95.f);
      const unsigned mh_ = pk2(m, 0.f) & 0xffffu;
      lds_st<u32x2>(q2s + h * QX_B, u32x2{q2o[h][0], mh_ | (pk2(0.f, m - bflo(mh_)) & 0xffff0000u)});
    }
    CSIG(C_QX);
    }
    if (kh2 != h0) cwait_<0, 4 * C_KVF0>(mh, 1);
    u32x4 p1 = {0, 0, 0, 0}; f32x4 e = {0.f, 0.f, 0.f, 0.f};
    f32x16 acc[4];
    u32x4 kA = lds_ld<u32x4>(ak1), kB = lds_ld<u32x4>(ak1 + TILE_B); u32x2 k2A = lds_ld<u32x2>(ak2), k2B = lds_ld<u32x2>(ak2 + TILE_B);
    u32x4 vA0 = lds_ld<u32x4>(av), vA1 = lds_ld<u32x4>(av + 2 * BLK_B);
    u32x4 vB1 = vA1;
    f32x16 sa;
#pragma unroll
    for (int n = 0; n < 4; ++n) { PIN(p1); acc[n] = MFMA16(BC8(p1), BC8(p1), zero); }
    CWAIT(C_QX, 4, 0);
    u32x4 q1[4]; u32x2 q2[4];
#pragma unroll
    for (int n = 0; n < 4; ++n) { const int hn = 2 * ((n + 2 * kh2) & 3); q1[n] = lds_ld<u32x4>(q1d + hn * QX_B); q2[n] = lds_ld<u32x2>(q2d + hn * QX_B); }
    asm volatile("s_waitcnt lgkmcnt(0)" : "+v"(q1[0]), "+v"(q1[1]), "+v"(q1[2]), "+v"(q1[3]), "+v"(q2[0]), "+v"(q2[1]), "+v"(q2[2]), "+v"(q2[3]), "+v"(vA0), "+v"(vA1), "+v"(vB1), "+v"(kA), "+v"(kB), "+v"(k2A), "+v"(k2B));
    sa = MFMA16(BC8(kA), BC8(q1[0]), zero); sa = MFMA8(BC4(k2A), BC4(q2[0]), sa);
    unsigned fs; int dl = KVSLOT(kh2, 1) - KVSLOT(kh2, 0), d2 = KVS_B, d3 = KVSLOT(kh2, 0) - KVSLOT(kh2, 2), need = 2, bound = 1 << 22;
    typedef __attribute__((ext_vector_type(16))) unsigned u32x16; typedef __attribute__((ext_vector_type(8))) unsigned u32x8;
    u32x16 q1a; u32x8 q2a_;
#pragma unroll
    for (int n = 0; n < 4; ++n) { q1a[4 * n] = q1[n][0]; q1a[4 * n + 1] = q1[n][1]; q1a[4 * n + 2] = q1[n][2]; q1a[4 * n + 3] = q1[n][3]; q2a_[2 * n] = q2[n][0]; q2a_[2 * n + 1] = q2[n][1]; }
    asm volatile(
      "s_nop 10\n"
      ".Latt_entry:\n"
      "1:\n"
      "v_mfma_f32_32x32x16_bf16 v[80:95], v[120:123], v[100:103], 0\n"
      "ds_read_b128 v[140:143], v158 offset:2560\n"
      "v_cvt_pk_bf16_f32 v154, v148, v149\n"
      "v_cvt_pk_bf16_f32 v155, v150, v151\n"
      "v_exp_f32 v64, v64\n"
      "v_exp_f32 v65, v65\n"
      "v_exp_f32 v66, v66\n"
      "v_exp_f32 v67, v67\n"
      "v_mfma_f32_32x32x16_bf16 v[48:63], v[144:147], v[152:155], v[48:63]\n"
      "ds_read_b128 v[144:147], v158 offset:3840\n"
      "v_cvt_pk_bf16_f32 v148, v64, v65\n"
      "v_cvt_pk_bf16_f32 v149, v66, v67\n"
      "v_exp_f32 v68, v68\n"
      "v_exp_f32 v69, v69\n"
      "v_exp_f32 v70, v70\n"
      "v_exp_f32 v71, v71\n"
      "v_mfma_f32_32x32x8_bf16 v[80:95], v[128:129], v[114:115], v[80:95]\n"
      "v_cvt_pk_bf16_f32 v150, v68, v69\n"
      "v_cvt_pk_bf16_f32 v151, v70, v71\n"
      "v_exp_f32 v72, v72\n"
      "v_exp_f32 v73, v73\n"
      "v_exp_f32 v74, v74\n"
      "v_exp_f32 v75, v75\n"
      "v_mfma_f32_32x32x16_bf16 v[0:15], v[132:135], v[148:151], v[0:15]\n"
      "ds_read_b32 v161, v159 offset:44\n"
      "v_cvt_pk_bf16_f32 v152, v72, v73\n"
      "v_cvt_pk_bf16_f32 v153, v74, v75\n"
      "v_exp_f32 v148, v76\n"
      "v_exp_f32 v149, v77\n"
      "v_exp_f32 v150, v78\n"
      "v_exp_f32 v151, v79\n"
      "v_mfma_f32_32x32x16_bf16 v[64:79], v[120:123], v[104:107], 0\n"
      "v_cvt_pk_bf16_f32 v154, v148, v149\n"
      "v_cvt_pk_bf16_f32 v155, v150, v151\n"
      "v_exp_f32 v80, v80\n"
      "v_exp_f32 v81, v81\n"
      "v_exp_f32 v82, v82\n"
      "v_exp_f32 v83, v83\n"
      "v_mfma_f32_32x32x16_bf16 v[0:15], v[136:139], v[152:155], v[0:15]\n"
      "v_cvt_pk_bf16_f32 v148, v80, v81\n"
      "v_cvt_pk_bf16_f32 v149, v82, v83\n"
      "v_exp_f32 v84, v84\n"
      "v_exp_f32 v85, v85\n"
      "v_exp_f32 v86, v86\n"
      "v_exp_f32 v87, v87\n"
      "v_mfma_f32_32x32x8_bf16 v[64:79], v[128:129], v[116:117], v[64:79]\n"
      "v_cvt_pk_bf16_f32 v150, v84, v85\n"
      "v_cvt_pk_bf16_f32 v151, v86, v87\n"
      "v_exp_f32 v88, v88\n"
      "v_exp_f32 v89, v89\n"
      "v_exp_f32 v90, v90\n"
      "v_exp_f32 v91, v91\n"
      "v_mfma_f32_32x32x16_bf16 v[16:31], v[132:135], v[148:151], v[16:31]\n"
      "s_waitcnt lgkmcnt(0)\n"
      "v_cvt_pk_bf16_f32 v152, v88, v89\n"
      "v_cvt_pk_bf16_f32 v153, v90, v91\n"
      "v_exp_f32 v148, v92\n"
      "v_exp_f32 v149, v93\n"
      "v_exp_f32 v150, v94\n"
      "v_exp_f32 v151, v95\n"
      "v_readfirstlane_b32 %4, v161\n"
      "v_add_u32 v156, %1, v156\n"
      "v_add_u32 v157, %1, v157\n"
      "v_add_u32 v158, %1, v158\n"
      "v_mfma_f32_32x32x16_bf16 v[80:95], v[120:123], v[108:111], 0\n"
      "ds_add_u32 v159, v160 offset:60\n"
      "v_cvt_pk_bf16_f32 v154, v148, v149\n"
      "v_cvt_pk_bf16_f32 v155, v150, v151\n"
      "v_exp_f32 v64, v64\n"
      "v_exp_f32 v65, v65\n"
      "v_exp_f32 v66, v66\n"
      "v_exp_f32 v67, v67\n"
      "v_mfma_f32_32x32x16_bf16 v[16:31], v[136:139], v[152:155], v[16:31]\n"
      "s_cmp_ge_u32 %4, %0\n"
      "v_cvt_pk_bf16_f32 v148, v64, v65\n"
      "v_cvt_pk_bf16_f32 v149, v66, v67\n"
      "v_exp_f32 v68, v68\n"
      "v_exp_f32 v69, v69\n"
      "v_exp_f32 v70, v70\n"
      "v_exp_f32 v71, v71\n"
      "v_mfma_f32_32x32x8_bf16 v[80:95], v[128:129], v[118:119], v[80:95]\n"
      "v_cvt_pk_bf16_f32 v150, v68, v69\n"
      "v_cvt_pk_bf16_f32 v151, v70, v71\n"
      "v_exp_f32 v72, v72\n"
      "v_exp_f32 v73, v73\n"
      "v_exp_f32 v74, v74\n"
      "v_exp_f32 v75, v75\n"
      "v_mfma_f32_32x32x16_bf16 v[32:47], v[132:135], v[148:151], v[32:47]\n"
      "s_cbranch_scc0 3f\n"
      "2:\n"
      "v_cvt_pk_bf16_f32 v152, v72, v73\n"
      "v_cvt_pk_bf16_f32 v153, v74, v75\n"
      "v_exp_f32 v148, v76\n"
      "v_exp_f32 v149, v77\n"
      "v_exp_f32 v150, v78\n"
      "v_exp_f32 v151, v79\n"
      "v_mfma_f32_32x32x16_bf16 v[64:79], v[124:127], v[96:99], 0\n"
      "ds_read_b128 v[120:123], v156 offset:0\n"
      "v_cvt_pk_bf16_f32 v154, v148, v149\n"
      "v_cvt_pk_bf16_f32 v155, v150, v151\n"
      "v_exp_f32 v80, v80\n"
      "v_exp_f32 v81, v81\n"
      "v_exp_f32 v82, v82\n"
      "v_exp_f32 v83, v83\n"
      "v_mfma_f32_32x32x16_bf16 v[32:47], v[136:139], v[152:155], v[32:47]\n"
      "ds_read_b64 v[128:129], v157 offset:0\n"
      "v_cvt_pk_bf16_f32 v148, v80, v81\n"
      "v_cvt_pk_bf16_f32 v149, v82, v83\n"
      "v_exp_f32 v84, v84\n"
      "v_exp_f32 v85, v85\n"
      "v_exp_f32 v86, v86\n"
      "v_exp_f32 v87, v87\n"
      "v_mfma_f32_32x32x8_bf16 v[64:79], v[130:131], v[112:113], v[64:79]\n"
      "v_cvt_pk_bf16_f32 v150, v84, v85\n"
      "v_cvt_pk_bf16_f32 v151, v86, v87\n"
      "v_exp_f32 v88, v88\n"
      "v_exp_f32 v89, v89\n"
      "v_exp_f32 v90, v90\n"
      "v_exp_f32 v91, v91\n"
      "v_mfma_f32_32x32x16_bf16 v[48:63], v[132:135], v[148:151], v[48:63]\n"
      "s_mov_b32 %4, %1\n"
      "v_cvt_pk_bf16_f32 v152, v88, v89\n"
      "v_cvt_pk_bf16_f32 v153, v90, v91\n"
      "v_exp_f32 v148, v92\n"
      "v_exp_f32 v149, v93\n"
      "v_exp_f32 v150, v94\n"
      "v_exp_f32 v151, v95\n"
      "v_mfma_f32_32x32x16_bf16 v[80:95], v[124:127], v[100:103], 0\n"
      "ds_read_b128 v[132:135], v158 offset:0\n"
      "v_cvt_pk_bf16_f32 v154, v148, v149\n"
      "v_cvt_pk_bf16_f32 v155, v150, v151\n"
      "v_exp_f32 v64, v64\n"
      "v_exp_f32 v65, v65\n"
      "v_exp_f32 v66, v66\n"
      "v_exp_f32 v67, v67\n"
      "v_mfma_f32_32x32x16_bf16 v[48:63], v[136:139], v[152:155], v[48:63]\n"
      "ds_read_b128 v[136:139], v158 offset:1280\n"
      "v_cvt_pk_bf16_f32 v148, v64, v65\n"
      "v_cvt_pk_bf16_f32 v149, v66, v67\n"
      "v_exp_f32 v68, v68\n"
      "v_exp_f32 v69, v69\n"
      "v_exp_f32 v70, v70\n"
      "v_exp_f32 v71, v71\n"
      "v_mfma_f32_32x32x8_bf16 v[80:95], v[130:131], v[114:115], v[80:95]\n"
      "v_cvt_pk_bf16_f32 v150, v68, v69\n"
      "v_cvt_pk_bf16_f32 v151, v70, v71\n"
      "v_exp_f32 v72, v72\n"
      "v_exp_f32 v73, v73\n"
      "v_exp_f32 v74, v74\n"
      "v_exp_f32 v75, v75\n"
      "v_mfma_f32_32x32x16_bf16 v[0:15], v[140:143], v[148:151], v[0:15]\n"
      "s_mov_b32 %1, %2\n"
      "v_cvt_pk_bf16_f32 v152, v72, v73\n"
      "v_cvt_pk_bf16_f32 v153, v74, v75\n"
      "v_exp_f32 v148, v76\n"
      "v_exp_f32 v149, v77\n"
      "v_exp_f32 v150, v78\n"
      "v_exp_f32 v151, v79\n"
      "v_mfma_f32_32x32x16_bf16 v[64:79], v[124:127], v[104:107], 0\n"
      "s_mov_b32 %2, %3\n"
      "v_cvt_pk_bf16_f32 v154, v148, v149\n"
      "v_cvt_pk_bf16_f32 v155, v150, v151\n"
      "v_exp_f32 v80, v80\n"
      "v_exp_f32 v81, v81\n"
      "v_exp_f32 v82, v82\n"
      "v_exp_f32 v83, v83\n"
      "v_mfma_f32_32x32x16_bf16 v[0:15], v[144:147], v[152:155], v[0:15]\n"
      "s_mov_b32 %3, %4\n"
      "v_cvt_pk_bf16_f32 v148, v80, v81\n"
      "v_cvt_pk_bf16_f32 v149, v82, v83\n"
      "v_exp_f32 v84, v84\n"
      "v_exp_f32 v85, v85\n"
      "v_exp_f32 v86, v86\n"
      "v_exp_f32 v87, v87\n"
      "v_mfma_f32_32x32x8_bf16 v[64:79], v[130:131], v[116:117], v[64:79]\n"
      "v_cvt_pk_bf16_f32 v150, v84, v85\n"
      "v_cvt_pk_bf16_f32 v151, v86, v87\n"
      "v_exp_f32 v88, v88\n"
      "v_exp_f32 v89, v89\n"
      "v_exp_f32 v90, v90\n"
      "v_exp_f32 v91, v91\n"
      "v_mfma_f32_32x32x16_bf16 v[16:31], v[140:143], v[148:151], v[16:31]\n"
      "s_add_u32 %0, %0, 1\n"
      "v_cvt_pk_bf16_f32 v152, v88, v89\n"
      "v_cvt_pk_bf16_f32 v153, v90, v91\n"
      "v_exp_f32 v148, v92\n"
      "v_exp_f32 v149, v93\n"
      "v_exp_f32 v150, v94\n"
      "v_exp_f32 v151, v95\n"
      "v_mfma_f32_32x32x16_bf16 v[80:95], v[124:127], v[108:111], 0\n"
      "v_cvt_pk_bf16_f32 v154, v148, v149\n"
      "v_cvt_pk_bf16_f32 v155, v150, v151\n"
      "v_exp_f32 v64, v64\n"
      "v_exp_f32 v65, v65\n"
      "v_exp_f32 v66, v66\n"
      "v_exp_f32 v67, v67\n"
      "v_mfma_f32_32x32x16_bf16 v[16:31], v[144:147], v[152:155], v[16:31]\n"
      "v_cvt_pk_bf16_f32 v148, v64, v65\n"
      "v_cvt_pk_bf16_f32 v149, v66, v67\n"
      "v_exp_f32 v68, v68\n"
      "v_exp_f32 v69, v69\n"
      "v_exp_f32 v70, v70\n"
      "v_exp_f32 v71, v71\n"
      "v_mfma_f32_32x32x8_bf16 v[80:95], v[130:131], v[118:119], v[80:95]\n"
      "v_cvt_pk_bf16_f32 v150, v68, v69\n"
      "v_cvt_pk_bf16_f32 v151, v70, v71\n"
      "v_exp_f32 v72, v72\n"
      "v_exp_f32 v73, v73\n"
      "v_exp_f32 v74, v74\n"
      "v_exp_f32 v75, v75\n"
      "v_mfma_f32_32x32x16_bf16 v[32:47], v[140:143], v[148:151], v[32:47]\n"
      "s_waitcnt lgkmcnt(0)\n"
      "v_cvt_pk_bf16_f32 v152, v72, v73\n"
      "v_cvt_pk_bf16_f32 v153, v74, v75\n"
      "v_exp_f32 v148, v76\n"
      "v_exp_f32 v149, v77\n"
      "v_exp_f32 v150, v78\n"
      "v_exp_f32 v151, v79\n"
      "v_mfma_f32_32x32x16_bf16 v[64:79], v[120:123], v[96:99], 0\n"
      "ds_read_b128 v[124:127], v156 offset:2560\n"
      "v_cvt_pk_bf16_f32 v154, v148, v149\n"
      "v_cvt_pk_bf16_f32 v155, v150, v151\n"
      "v_exp_f32 v80, v80\n"
      "v_exp_f32 v81, v81\n"
      "v_exp_f32 v82, v82\n"
      "v_exp_f32 v83, v83\n"
      "v_mfma_f32_32x32x16_bf16 v[32:47], v[144:147], v[152:155], v[32:47]\n"
      "ds_read_b64 v[130:131], v157 offset:2560\n"
      "v_cvt_pk_bf16_f32 v148, v80, v81\n"
      "v_cvt_pk_bf16_f32 v149, v82, v83\n"
      "v_exp_f32 v84, v84\n"
      "v_exp_f32 v85, v85\n"
      "v_exp_f32 v86, v86\n"
      "v_exp_f32 v87, v87\n"
      "v_mfma_f32_32x32x8_bf16 v[64:79], v[128:129], v[112:113], v[64:79]\n"
      "v_cvt_pk_bf16_f32 v150, v84, v85\n"
      "v_cvt_pk_bf16_f32 v151, v86, v87\n"
      "v_exp_f32 v88, v88\n"
      "v_exp_f32 v89, v89\n"
      "v_exp_f32 v90, v90\n"
      "v_exp_f32 v91, v91\n"
      "v_mfma_f32_32x32x16_bf16 v[48:63], v[140:143], v[148:151], v[48:63]\n"
      "s_cmp_lg_u32 %0, 18\n"
      "v_cvt_pk_bf16_f32 v152, v88, v89\n"
      "v_cvt_pk_bf16_f32 v153, v90, v91\n"
      "v_exp_f32 v148, v92\n"
      "v_exp_f32 v149, v93\n"
      "v_exp_f32 v150, v94\n"
      "v_exp_f32 v151, v95\n"
      "s_cbranch_scc1 1b\n"
      "s_branch 4f\n"
      "3:\n"
      "ds_read_b32 v161, v159 offset:44\n"
      "s_waitcnt lgkmcnt(0)\n"
      "v_readfirstlane_b32 %4, v161\n"
      "s_sub_u32 %5, %5, 1\n"
      "s_cbranch_scc1 2b\n"
      "s_cmp_ge_u32 %4, %0\n"
      "s_cbranch_scc1 2b\n"
      "s_branch 3b\n"
      "4:\n"
      "s_cbranch_execz .Latt_dry_ret\n"
      "v_cvt_pk_bf16_f32 v154, v148, v149\n"
      "v_cvt_pk_bf16_f32 v155, v150, v151\n"
      "s_waitcnt lgkmcnt(0)\n"
      "s_nop 0\n"
      "v_mfma_f32_32x32x16_bf16 v[48:63], v[144:147], v[152:155], v[48:63]\n"
      "s_nop 11\n"
      : "+{s40}"(need), "+{s41}"(dl), "+{s42}"(d2), "+{s43}"(d3), "=&{s44}"(fs), "+{s45}"(bound),
        "+{v[0:15]}"(acc[0]), "+{v[16:31]}"(acc[1]), "+{v[32:47]}"(acc[2]), "+{v[48:63]}"(acc[3]), "+{v[64:79]}"(sa),
        "+{v[120:123]}"(kA), "+{v[124:127]}"(kB), "+{v[128:129]}"(k2A), "+{v[130:131]}"(k2B), "+{v[132:135]}"(vA0), "+{v[136:139]}"(vA1), "+{v[144:147]}"(vB1),
        "+{v[148:151]}"(e), "+{v[152:155]}"(p1), "+{v156}"(ak1), "+{v157}"(ak2), "+{v158}"(av)
      : "{v[96:111]}"(q1a), "{v[112:119]}"(q2a_), "{v159}"(mh), "{v160}"(one)
      : "memory", "scc", "v80", "v81", "v82", "v83", "v84", "v85", "v86", "v87", "v88", "v89", "v90", "v91", "v92", "v93", "v94", "v95", "v140", "v141", "v142", "v143", "v161");
    PRIO(2);
    {
      LANES();
      unsigned cw = L_CMB + (4 * (kh2 ^ 1) + qh) * 2560 + lane * 16, cr = L_CMB + (4 * kh2 + qh) * 2560 + lane * 16, c3 = hi ? OOB : 2048u;
      unsigned cw3 = cw + c3, cr3 = cr + c3, sg = L_STG_B + (qh * 32 + r32) * 160 + kh2 * 72 + hi * 8, sg5 = hi ? OOB : sg;
      unsigned la[3], go[3];
#pragma unroll
      for (int i = 0; i < 3; ++i) { piece_addr(wid + 4 * i, lane, 160, la[i], go[i]); if (i == 2 && wid) go[i] = OOB; PIN(la[i]); PIN(go[i]); }
      PIN(cw); PIN(cr); PIN(cw3); PIN(cr3); PIN(sg); PIN(sg5);
      CSYNC(C_JOIN, 4, 0);
#pragma unroll
      for (int j = 2; j < 4; ++j) {
        lds_st<f32x4>(cw + (j - 2) * 5120, f32x4{acc[j][0], acc[j][1], acc[j][2], acc[j][3]}); lds_st<f32x4>(cw + (j - 2) * 5120 + 1024, f32x4{acc[j][4], acc[j][5], acc[j][6], acc[j][7]});
        lds_st<f32x4>(cw3 + (j - 2) * 5120, f32x4{acc[j][8], acc[j][9], acc[j][10], acc[j][11]});
      }
      CSYNC(C_JOIN, 8, 0);
      f32x4 v[2][3];
#pragma unroll
      for (int j = 0; j < 2; ++j) { v[j][0] = lds_ld<f32x4>(cr + j * 5120); v[j][1] = lds_ld<f32x4>(cr + j * 5120 + 1024); v[j][2] = lds_ld<f32x4>(cr3 + j * 5120); }
      SB();
#pragma unroll
      for (int j = 0; j < 2; ++j) {
        float of[11];
#pragma unroll
        for (int i = 0; i < 11; ++i) of[i] = acc[j][i] + v[j][i >> 2][i & 3];
        float l = lowhalf(of[10]);
        float asum = 0.f;
#pragma unroll
        for (int i = 0; i < 10; ++i) asum += fabsf(of[i]);
        const bool ok = (l > 1.0e-30f) && (l < 1.0e37f) && (asum < 1.0e37f) && (asum > 2.0e-34f);
        if (__builtin_expect(__builtin_amdgcn_readfirstlane(__any(!ok)), 0)) {
          const __amdgpu_buffer_rsrc_t ri = mk_rs(ws + WS_IMG, 64 * TILE_B);
          LANES(); KVOFFS();
          const u32x4 q1o = {q1a[4 * j], q1a[4 * j + 1], q1a[4 * j + 2], q1a[4 * j + 3]};
          const u32x2 q2o = {q2a_[2 * j], 0u};
#define LDK1(t) __builtin_amdgcn_raw_buffer_load_b128(ri, vk1, (t) * TILE_B, 16)
#define LDK2(t) __builtin_amdgcn_raw_buffer_load_b64(ri, vk2, (t) * TILE_B, 16)
#define LDV0(t) __builtin_amdgcn_raw_buffer_load_b128(ri, vv, (t) * TILE_B, 16)
#define LDV1(t) __builtin_amdgcn_raw_buffer_load_b128(ri, vv + 2 * BLK_B, (t) * TILE_B, 16)
          float m = -3.0e38f;
#pragma unroll 1
          for (int t = 0; t < 64; ++t) { f32x16 s = MFMA16(BC8(LDK1(t)), BC8(q1o), zero); s = MFMA8(BC4(LDK2(t)), BC4(q2o), s);
#pragma unroll
            for (int i = 0; i < 16; i += 2) m = fmaxf(fmaxf(m, s[i]), s[i + 1]); }
          m = swapmax(m);
          f32x16 os = zero;
#pragma unroll 1
          for (int t = 0; t < 64; ++t) {
            f32x16 s = MFMA16(BC8(LDK1(t)), BC8(q1o), zero); s = MFMA8(BC4(LDK2(t)), BC4(q2o), s);
            u32x4 p0, p1_;
#pragma unroll
            for (int i = 0; i < 4; ++i) { p0[i] = pk2(EX(s[2 * i] - m), EX(s[2 * i + 1] - m)); p1_[i] = pk2(EX(s[8 + 2 * i] - m), EX(s[9 + 2 * i] - m)); }
            os = MFMA16(BC8(LDV0(t)), BC8(p0), os); os = MFMA16(BC8(LDV1(t)), BC8(p1_), os);
          }
#pragma unroll
          for (int i = 0; i < 11; ++i) of[i] = os[i];
          l = lowhalf(of[10]);
        }
        const float rl = __builtin_amdgcn_rcpf(l);
#pragma unroll
        for (int i = 0; i < 5; ++i)
          lds_st<unsigned>((i < 4 ? sg : sg5) + j * 36 + (i >> 1) * 16 + (i & 1) * 4, pk2(of[2 * i] * rl, of[2 * i + 1] * rl));
      }
      CSYNC(C_JOIN, 12, 0);
      const __amdgpu_buffer_rsrc_t ro = mk_rs(ws + WS_OB + (size_t)rb * 73728 + hg * 2304, 73728);
#pragma unroll
      for (int i = 0; i < 3; ++i) __builtin_amdgcn_raw_buffer_store_b128(lds_ld<u32x4>(L_STG_B + la[i]), ro, go[i], 0, 16);
      if (b == 0 && wid == 0 && lane == 0) __hip_atomic_store((GAS unsigned*)(ws + WS_EPOCH), tag, __ATOMIC_RELAXED, __HIP_MEMORY_SCOPE_AGENT);
      WAITV(0);
      CSIG(C_JOIN);
      if (wid == 0) {
        CWAIT(C_JOIN, 16, 0);
        if (lane == 0) __hip_atomic_store(FLAG(WS_OBFLAG), tag, __ATOMIC_RELAXED, __HIP_MEMORY_SCOPE_AGENT);
      }
    }
  } else {
    {
    LANES();
    const int cs = qh, p0 = kh2 * 5;
    const int row = (oq + cs) * 8 + (i16 & 7);
    unsigned xa = L_XB + row * 128 + (((2 * g4) ^ ((row >> 1) & 7)) << 4) + p0 * 8192, xb = xa ^ 16;
    unsigned x4 = kh2 ? OOB : xa + 32768, x5 = kh2 ? OOB : xb + 32768;
    unsigned sel = (i16 & 8) ? 0x07060302u : 0x05040100u;
    unsigned wa[5], w2[5];
#pragma unroll
    for (int i = 0; i < 5; ++i) { int kc = p0 + i + koff; kc = kc >= 9 ? kc - 9 : kc; kc = kc >= 9 ? kc - 9 : kc; w2[i] = L_R0 + (8 * g4 + tq) * 72 + kc * 2304 + 64; wa[i] = w2[i] - 64 + tp * 8; PIN(wa[i]); PIN(w2[i]); }
    const int gs = (i16 & 8) ? 4 : 0;
    unsigned dd[3];
#pragma unroll
    for (int j = 0; j < 3; ++j) { const int G = 4 * j + g4 - gs; dd[j] = (G >= 0 && G < 5 && 4 * j + g4 < 9) ? (unsigned)(L_D + (kh2 * 2 + cs) * 1280 + i16 * 80 + G * 16) : OOB; PIN(dd[j]); }
    PIN(xa); PIN(xb); PIN(x4); PIN(x5); PIN(sel);
    PRIO(2);
    CWAIT(C_KVIN, 4, 0);
    u32x4 x0[5], x1[5]; bf16x8 wf[5][3];
#pragma unroll
    for (int i = 0; i < 5; ++i) { x0[i] = lds_ld<u32x4>(i < 4 ? xa + i * 8192 : x4); x1[i] = lds_ld<u32x4>(i < 4 ? xb + i * 8192 : x5);
      wf[i][0] = trd2(wa[i], 288); wf[i][1] = trd2(wa[i] + 32, 288); wf[i][2] = trd2(w2[i], 288); }
    tag = __builtin_amdgcn_readfirstlane(lds_ld<unsigned>(CA(C_TAG))) + 1u;
    SB();
    f32x4 d[3];
#pragma unroll
    for (int j = 0; j < 3; ++j) d[j] = f32x4{0.f, 0.f, 0.f, 0.f};
#pragma unroll
    for (int i = 0; i < 5; ++i) {
      u32x4 xf = {__builtin_amdgcn_perm(x0[i][1], x0[i][0], sel), __builtin_amdgcn_perm(x0[i][3], x0[i][2], sel), __builtin_amdgcn_perm(x1[i][1], x1[i][0], sel), __builtin_amdgcn_perm(x1[i][3], x1[i][2], sel)};
#pragma unroll
      for (int j = 0; j < 3; ++j) d[j] = MF16(wf[i][j], BC8(xf), d[j]);
    }
#pragma unroll
    for (int j = 0; j < 3; ++j) lds_st<f32x4>(dd[j], d[j]);
    CSIG(C_KVP);
    PRIO(0);
    }
    PRIO(1);
    const int a = wid - 4;
    {
      LANES();
      f32x4 ac[17];
#pragma unroll
      for (int i = 0; i < 17; ++i) { ac[i] = f32x4{0.f, 0.f, 0.f, 0.f}; }
      unsigned wa[5], xa[2], xe[2];
#pragma unroll
      for (int n = 0; n < 5; ++n) { const int nt = n < 2 ? 2 * a + n : (n == 2 ? 9 + a : (n == 3 ? 8 : 13)); int gm = 4 * nt + tp; gm = gm > 53 ? 53 : gm; const int sg = (gm * 57) >> 10;
        wa[n] = (unsigned)((8 * g4 + tq) * 144 + (sg == 2 ? 0 : sg + 1) * 9216 + (gm - sg * 18) * 8); PIN(wa[n]); }
#pragma unroll
      for (int s = 0; s < 2; ++s) { xa[s] = (unsigned)(SLOT_X + i16 * 128 + (((4 * s + g4) ^ ((i16 >> 1) & 7)) << 4)); xe[s] = xa[s] + (a + 1) * 2048; PIN(xa[s]); PIN(xe[s]); }
      int sl = 0;
      unsigned vf = CA(C_AF0) + (lane & 1) * 4, ma = mb + 4 * a; PIN(vf); PIN(ma);
      bf16x8 wf[5], xf[2][6];
#define VMF(acc, a, b) asm volatile("v_mfma_f32_16x16x32_bf16 %0, %1, %2, %0" : "+v"(acc) : "v"(a), "v"(b))
#define XREAD(s, bf) do { _Pragma("unroll") for (int m = 0; m < 5; ++m) xf[bf][m] = lds_ld<bf16x8>(xa[s] + m * 2048); xf[bf][5] = lds_ld<bf16x8>(xe[s]); SB(); } while (0)
#define WRD(n, s) do { wf[n] = trd2(wa[n] + (s) * 4608, 576); SB(); } while (0)
#define AMMA(bf, sn) do { \
          _Pragma("unroll") for (int m = 0; m < 5; ++m) VMF(ac[m], wf[0], xf[bf][m]); SB(); WRD(0, sn); \
          _Pragma("unroll") for (int m = 0; m < 5; ++m) VMF(ac[5 + m], wf[1], xf[bf][m]); SB(); WRD(1, sn); \
          VMF(ac[14], wf[3], xf[bf][5]); if (a == 0) VMF(ac[16], wf[3], xf[bf][0]); SB(); WRD(3, sn); VMF(ac[15], wf[4], xf[bf][5]); SB(); WRD(4, sn); \
          _Pragma("unroll") for (int m = 1; m < 5; ++m) VMF(ac[9 + m], wf[2], xf[bf][m]); SB(); WRD(2, sn); } while (0)
      cwaitn<2>(vf, 1);
      WRD(0, 0); XREAD(0, 0); WRD(1, 0); WRD(3, 0); WRD(4, 0); WRD(2, 0);
#pragma unroll 1
      for (int c = 0; c < 9; ++c) {
        PRIO(0);
        XREAD(1, 1); AMMA(0, 1);
        const int dl = sl == 2 ? -2 * SLOT_B : SLOT_B; sl = sl == 2 ? 0 : sl + 1;
        PRIO(1);
        CSIGV(ma, 4 * C_AD0);
#pragma unroll
        for (int n = 0; n < 5; ++n) { wa[n] += dl; PIN(wa[n]); }
#pragma unroll
        for (int s = 0; s < 2; ++s) { xa[s] += dl; xe[s] += dl; PIN(xa[s]); PIN(xe[s]); }
        cwaitn<2>(vf, c < 8 ? c + 2 : 9);
        PRIO(0);
        XREAD(0, 0);
        AMMA(1, 0);
      }
      PRIO(1);
      cwaitn<1>(CA(C_AD0) + (lane & 3) * 4, 9);
      const unsigned fb = i16 * F32_S + g4 * 16, f0 = fb + a * 128, fq = fb + (9 + a) * 64, fe = fb + (a + 1) * 16 * F32_S;
#pragma unroll
      for (int m = 0; m < 5; ++m) { lds_st<f32x4>(f0 + m * 16 * F32_S, ac[m]); lds_st<f32x4>(f0 + 64 + m * 16 * F32_S, ac[5 + m]); if (m) lds_st<f32x4>(fq + m * 16 * F32_S, ac[9 + m]); }
      lds_st<f32x4>(fe + 8 * 64, ac[14]); lds_st<f32x4>(fe + 13 * 64, ac[15]); if (a == 0) lds_st<f32x4>(fb + 8 * 64, ac[16]);
      CSYNC(C_AX, 4, 1);
    }
    {
      LANES();
      const unsigned r16 = (2 + lane) * F32_S + a * 72 + (a & 1) * 8, r8 = (2 + lane) * F32_S + a * 72 + ((a & 1) ? 0 : 64);
      f32x4 q[4], x[4][4]; f32x2 q2, x2[4];
#define LDV(d4, d2, w, off) do { _Pragma("unroll") for (int i = 0; i < 4; ++i) d4[i] = lds_ld<f32x4>(r16 + (14 - 2 * (w)) * F32_S + (off) + i * 16); d2 = lds_ld<f32x2>(r8 + (14 - 2 * (w)) * F32_S + (off)); } while (0)
#define V4(v) "v"(v[0]), "v"(v[1]), "v"(v[2]), "v"(v[3])
#define V2(v) "v"(v[0]), "v"(v[1])
      q2 = lds_ld<f32x2>(r8 + 14 * F32_S + 576);
#pragma unroll
      for (int w = 0; w < 4; ++w) x2[w] = lds_ld<f32x2>(r8 + (14 - 2 * w) * F32_S);
#pragma unroll
      for (int i = 0; i < 4; ++i) { q[i] = lds_ld<f32x4>(r16 + 14 * F32_S + 576 + i * 16);
#pragma unroll
        for (int w = 0; w < 4; ++w) x[w][i] = lds_ld<f32x4>(r16 + (14 - 2 * w) * F32_S + i * 16); }
      SB();
      float lg[8];
#pragma unroll
      for (int h = 0; h < 2; ++h) {
        const int w0 = h ? 0 : 4, off = h ? 288 : 0;
        asm("v_mul_f32 %0, %4, %6\n\tv_mul_f32 %1, %4, %8\n\tv_mul_f32 %2, %4, %10\n\tv_mul_f32 %3, %4, %12\n\tv_fmac_f32 %0, %5, %7\n\tv_fmac_f32 %1, %5, %9\n\tv_fmac_f32 %2, %5, %11\n\tv_fmac_f32 %3, %5, %13" : "=&v"(lg[4 * h]), "=&v"(lg[4 * h + 1]), "=&v"(lg[4 * h + 2]), "=&v"(lg[4 * h + 3]) : V2(q2), V2(x2[0]), V2(x2[1]), V2(x2[2]), V2(x2[3]));
        SB();
#pragma unroll
        for (int w = 0; w < 4; ++w) x2[w] = lds_ld<f32x2>(r8 + (14 - 2 * (w0 + w)) * F32_S + off);
        SB();
#pragma unroll
        for (int i = 0; i < 4; ++i) {
          asm("v_fmac_f32 %0, %4, %8\n\tv_fmac_f32 %1, %4, %12\n\tv_fmac_f32 %2, %4, %16\n\tv_fmac_f32 %3, %4, %20\n\tv_fmac_f32 %0, %5, %9\n\tv_fmac_f32 %1, %5, %13\n\tv_fmac_f32 %2, %5, %17\n\tv_fmac_f32 %3, %5, %21\n\tv_fmac_f32 %0, %6, %10\n\tv_fmac_f32 %1, %6, %14\n\tv_fmac_f32 %2, %6, %18\n\tv_fmac_f32 %3, %6, %22\n\tv_fmac_f32 %0, %7, %11\n\tv_fmac_f32 %1, %7, %15\n\tv_fmac_f32 %2, %7, %19\n\tv_fmac_f32 %3, %7, %23" : "+v"(lg[4 * h]), "+v"(lg[4 * h + 1]), "+v"(lg[4 * h + 2]), "+v"(lg[4 * h + 3]) : V4(q[i]), V4(x[0][i]), V4(x[1][i]), V4(x[2][i]), V4(x[3][i]));
          SB();
#pragma unroll
          for (int w = 0; w < 4; ++w) x[w][i] = lds_ld<f32x4>(r16 + (14 - 2 * (w0 + w)) * F32_S + off + i * 16);
          SB();
        }
      }
      if (rb == 0) {
#pragma unroll
        for (int w = 1; w < 8; ++w) lg[w] = lane < 2 * w ? -4.242640687e9f : lg[w];
      }
      const float mx = fmaxf(fmaxf(fmaxf(lg[0], lg[1]), fmaxf(lg[2], lg[3])), fmaxf(fmaxf(lg[4], lg[5]), fmaxf(lg[6], lg[7])));
      const float nm = -mx * QSCALE;
      float l = 0.f;
#pragma unroll
      for (int w = 0; w < 8; ++w) { lg[w] = EX(__builtin_fmaf(lg[w], QSCALE, nm)); l += lg[w]; }
      float rl = __builtin_amdgcn_rcpf(l);
      asm volatile("s_nop 1" : "+v"(rl), "+v"(lg[7]));
      float o[18];
#define O9(c, b) c(o[b]), c(o[b + 1]), c(o[b + 2]), c(o[b + 3]), c(o[b + 4]), c(o[b + 5]), c(o[b + 6]), c(o[b + 7]), c(o[b + 8])
#pragma unroll
      for (int w = 0; w < 8; ++w) {
        const int r = w & 3;
        if (w == 0) {
          asm("v_mul_f32 %0, %9, %10\n\tv_mul_f32 %1, %9, %11\n\tv_mul_f32 %2, %9, %12\n\tv_mul_f32 %3, %9, %13\n\tv_mul_f32 %4, %9, %14\n\tv_mul_f32 %5, %9, %15\n\tv_mul_f32 %6, %9, %16\n\tv_mul_f32 %7, %9, %17\n\tv_mul_f32 %8, %9, %18" : O9("=&v", 0) : "v"(lg[w]), V4(x[r][0]), V4(x[r][1]), "v"(x[r][2][0]));
          asm("v_mul_f32 %0, %9, %10\n\tv_mul_f32 %1, %9, %11\n\tv_mul_f32 %2, %9, %12\n\tv_mul_f32 %3, %9, %13\n\tv_mul_f32 %4, %9, %14\n\tv_mul_f32 %5, %9, %15\n\tv_mul_f32 %6, %9, %16\n\tv_mul_f32 %7, %9, %17\n\tv_mul_f32 %8, %9, %18" : O9("=&v", 9) : "v"(lg[w]), "v"(x[r][2][1]), "v"(x[r][2][2]), "v"(x[r][2][3]), V4(x[r][3]), V2(x2[r]));
        } else {
          asm("v_fmac_f32 %0, %9, %10\n\tv_fmac_f32 %1, %9, %11\n\tv_fmac_f32 %2, %9, %12\n\tv_fmac_f32 %3, %9, %13\n\tv_fmac_f32 %4, %9, %14\n\tv_fmac_f32 %5, %9, %15\n\tv_fmac_f32 %6, %9, %16\n\tv_fmac_f32 %7, %9, %17\n\tv_fmac_f32 %8, %9, %18" : O9("+v", 0) : "v"(lg[w]), V4(x[r][0]), V4(x[r][1]), "v"(x[r][2][0]));
          asm("v_fmac_f32 %0, %9, %10\n\tv_fmac_f32 %1, %9, %11\n\tv_fmac_f32 %2, %9, %12\n\tv_fmac_f32 %3, %9, %13\n\tv_fmac_f32 %4, %9, %14\n\tv_fmac_f32 %5, %9, %15\n\tv_fmac_f32 %6, %9, %16\n\tv_fmac_f32 %7, %9, %17\n\tv_fmac_f32 %8, %9, %18" : O9("+v", 9) : "v"(lg[w]), "v"(x[r][2][1]), "v"(x[r][2][2]), "v"(x[r][2][3]), V4(x[r][3]), V2(x2[r]));
        }
        SB();
        if (w < 4) { LDV(x[r], x2[r], w + 4, 288); SB(); }
      }
      const unsigned sg = L_STG_A + lane * 144 + a * 36 + (a & 1) * 4, s8 = L_STG_A + lane * 144 + a * 36 + ((a & 1) ? 0 : 32);
#pragma unroll
      for (int j = 0; j < 9; ++j) lds_st<unsigned>(j < 8 ? sg + j * 4 : s8, pk2(fmul(o[2 * j], rl), fmul(o[2 * j + 1], rl)));
    }
    CSYNC(C_AX, 8, 1);
    {
      const __amdgpu_buffer_rsrc_t ro = mk_rs(ws + WS_OA + (size_t)rb * 73728 + hg * 2304, 73728);
#pragma unroll
      for (int i = 0; i < 3; ++i) if (i < 2 || a == 0) { unsigned la, go; piece_addr(a + 4 * i, lane0, 144, la, go); __builtin_amdgcn_raw_buffer_store_b128(lds_ld<u32x4>(L_STG_A + la), ro, go, 0, 16); }
    }
    WAITV(0);
    CSIG(C_OAF);
    if (wid == 4) { CWAIT(C_OAF, 4, 1); if (lane0 == 0) __hip_atomic_store(FLAG(WS_OAFLAG), tag, __ATOMIC_RELAXED, __HIP_MEMORY_SCOPE_AGENT); }
    {
      LANES();
      const __amdgpu_buffer_rsrc_t ro = mk_rs(ws + WS_OA + (size_t)rb * 73728 + a * 18432, 18432);
      unsigned wa = L_WO + (8 * g4 + tq) * 144 + tp * 8, w4 = tp < 2 ? wa + 128 : wa + 112;
      unsigned wb = wa + L_WA5, w5 = w4 + L_WA5; PIN(wa); PIN(w4); PIN(wb); PIN(w5);
      CWAIT(C_WO, 2, 4);
      u32x4 of[18];
#pragma unroll
      for (int i = 0; i < 18; ++i) of[i] = __builtin_amdgcn_raw_buffer_load_b128(ro, lane * 16 + (i & 3) * 1024, (i >> 2) * 4096, 16);
      PRIO(0);
      bf16x8 wf[2][5]; f32x4 oc[5];
#define OREAD(i, bf) do { const unsigned p_ = (i) < 10 ? wa + (i) * 4608 : wb + ((i) - 10) * 4608, r_ = (i) < 10 ? w4 + (i) * 4608 : w5 + ((i) - 10) * 4608; \
        _Pragma("unroll") for (int n = 0; n < 4; ++n) wf[bf][n] = trd2(p_ + n * 32, 576); wf[bf][4] = trd2(r_, 576); SB(); } while (0)
      OREAD(0, 0); OREAD(1, 1);
#pragma unroll
      for (int n = 0; n < 5; ++n) asm volatile("v_mfma_f32_16x16x32_bf16 %0, %1, %2, 0" : "=&v"(oc[n]) : "v"(wf[0][n]), "v"(of[0]));
      SB();
#pragma unroll
      for (int i = 1; i < 18; ++i) {
        if (i == 9) CSIG(C_AX);
        if (i < 17) OREAD(i + 1, (i + 1) & 1);
#pragma unroll
        for (int n = 0; n < 5; ++n) VMF(oc[n], wf[i & 1][n], of[i]);
        SB();
      }
      PRIO(1);
      asm volatile("s_nop 7" : "+v"(oc[0]), "+v"(oc[1]), "+v"(oc[2]), "+v"(oc[3]), "+v"(oc[4]));
      const __amdgpu_buffer_rsrc_t rt = mk_rs(A.out + (size_t)(rb * 64 + a * 16) * (2 * D) + hg * 72, 16 * 2 * RB);
      const unsigned tv = i16 * 2 * RB + g4 * 8;
#pragma unroll
      for (int n = 0; n < 5; ++n) __builtin_amdgcn_raw_buffer_store_b64(u32x2{pk2(oc[n][0], oc[n][1]), pk2(oc[n][2], oc[n][3])}, rt, (n < 4 || g4 < 2) ? tv + n * 32 : OOB, 0, 0);
    }
    PRIO(0);
  }
  LANES();
  const int mq = wid & 3, kh = wid >> 2;
  const unsigned wa = L_WO + (kh * 288 + 8 * g4 + tq) * 144 + tp * 8, w4 = tp < 2 ? wa + 128 : wa + 112;
  bf16x8 wf[4][5];
#define WREAD(ks) do { _Pragma("unroll") for (int n = 0; n < 5; ++n) wf[(ks) & 3][n] = trd2((n < 4 ? wa + n * 32 : w4) + (ks) * 4608, 576); SB(); } while (0)
  CWAIT(C_WO, 4, 1);
  WREAD(0); WREAD(1); WREAD(2); WREAD(3);
  {
    if (wid == 0) wait_flags<2>(ws + WS_OBFLAG + rb * 256, tag, lane);
    asm volatile("s_barrier" ::: "memory");
    const __amdgpu_buffer_rsrc_t ro = mk_rs(ws + WS_OB + (size_t)rb * 73728 + mq * 18432 + kh * 9216, 9216);
    u32x4 of[9];
#pragma unroll
    for (int ks = 0; ks < 9; ++ks) of[ks] = __builtin_amdgcn_raw_buffer_load_b128(ro, lane * 16 + (ks & 3) * 1024, (ks >> 2) * 4096, 16);
    f32x4 oc[5];
#pragma unroll
    for (int n = 0; n < 5; ++n) oc[n] = f32x4{0.f, 0.f, 0.f, 0.f};
#pragma unroll
    for (int ks = 0; ks < 9; ++ks) {
#pragma unroll
      for (int n = 0; n < 5; ++n) oc[n] = MF16(wf[ks & 3][n], BC8(of[ks]), oc[n]);
      SB();
      if (ks + 4 < 9) WREAD(ks + 4);
    }
    if (kh) {
#pragma unroll
      for (int n = 0; n < 5; ++n) lds_st<f32x4>(L_RED + (mq * 5 + n) * 1024 + lane * 16, oc[n]);
    }
    BAR();
    if (!kh) {
      const __amdgpu_buffer_rsrc_t rt = mk_rs(A.out + (size_t)(rb * 64 + mq * 16) * (2 * D) + D + hg * 72, 16 * 2 * RB - RB);
      const unsigned tv = i16 * 2 * RB + g4 * 8;
#pragma unroll
      for (int n = 0; n < 5; ++n) { const f32x4 v = oc[n] + lds_ld<f32x4>(L_RED + (mq * 5 + n) * 1024 + lane * 16);
        __builtin_amdgcn_raw_buffer_store_b64(u32x2{pk2(v[0], v[1]), pk2(v[2], v[3])}, rt, (n < 4 || g4 < 2) ? tv + n * 32 : OOB, 0, 0); }
    }
  }
}

extern "C" void kernel_launch(void* const* d_in, const int* in_sizes, int n_in, void* d_out, int out_size, void* d_ws, size_t ws_size, hipStream_t stream) {
  Args a;
  a.Ax = (const u16*)d_in[0]; a.Awqkv = (const u16*)d_in[1]; a.Awo = (const u16*)d_in[2];
  a.Bx = (const u16*)d_in[3]; a.Bwq = (const u16*)d_in[4]; a.Bwk = (const u16*)d_in[5]; a.Bwv = (const u16*)d_in[6]; a.Bwo = (const u16*)d_in[7];
  a.ws = (char*)d_ws; a.out = (u16*)d_out;
  (void)hipFuncSetAttribute((const void*)k_fused, hipFuncAttributeMaxDynamicSharedMemorySize, 160 * 1024);
  k_fused<<<256, 768, 160 * 1024, stream>>>(a);
}
```
